```python
import torch

def _softmax(x, axis=-1):
    return torch.softmax(x, dim=axis)
def _gelu(x):
    return torch.nn.functional.gelu(x, approximate="tanh")
def _silu(x):
    return torch.nn.functional.silu(x)
def _sigmoid(x):
    return torch.sigmoid(x)
def _layernorm(x, axis=-1, eps=1e-5):
    m = x.mean(dim=axis, keepdim=True)
    v = x.var(dim=axis, keepdim=True, unbiased=False)
    return (x - m) / torch.sqrt(v + eps)
def _scatter_add(tgt, idx, src, axis=0):
    return tgt.clone().index_add_(axis, idx, src)
def _segment_sum(src, seg, n):
    out = torch.zeros((n,) + src.shape[1:], dtype=src.dtype)
    return out.index_add_(0, seg, src)
def _pad(x, pw):
    flat = tuple(v for pair in reversed(pw) for v in pair)
    return torch.nn.functional.pad(x, flat)

kernel_name = 'gated_mix__linear_kernel_hedgehog__dense_activation_relu__prompt'

def setup_inputs():

    _w = (lambda a: (lambda t: t.to(torch.bfloat16) if t.is_floating_point() else t)(torch.as_tensor(a)))
    def _A_setup():
        B, S, D, H = 1, 2048, 512, 32
        Dh = D // H
        return {
            "x": _w((lambda *s: torch.randn(*s, dtype=torch.bfloat16) * 0.05)(B, S, D)),
            "wqkv": _w((lambda *s: torch.randn(*s, dtype=torch.bfloat16) * 0.8)(D, 3 * D)),
            "w_phi": _w((lambda *s: torch.randn(*s, dtype=torch.bfloat16) * 0.8)(H, Dh, Dh) * (Dh ** -0.5)),
            "wo": _w((lambda *s: torch.randn(*s, dtype=torch.bfloat16) * 0.2)(D, D)),
        }
    _A_in = _A_setup()

    def _B_setup():
        B, S, D, Dff = 1, 2048, 512, 1792
        return {
            "x": _w((lambda *s: torch.randn(*s, dtype=torch.bfloat16) * 0.05)(B, S, D)),
            "w1": _w((lambda *s: torch.randn(*s, dtype=torch.bfloat16) * 0.05)(D, Dff)),
            "w2": _w((lambda *s: torch.randn(*s, dtype=torch.bfloat16) * 0.05)(Dff, D)),
        }
    _B_in = _B_setup()

    return {
        "A_x": _A_in["x"],
        "A_wqkv": _A_in["wqkv"],
        "A_w_phi": _A_in["w_phi"],
        "A_wo": _A_in["wo"],
        "B_x": _B_in["x"],
        "B_w1": _B_in["w1"],
        "B_w2": _B_in["w2"],
        "gate_w": _w((lambda *s: torch.randn(*s, dtype=torch.bfloat16) * 0.05)(512) * 16.0),
    }

def reference(A_x, A_wqkv, A_w_phi, A_wo, B_x, B_w1, B_w2, gate_w):
    def _A_ref(x, wqkv, w_phi, wo):
        B, S, D = x.shape
        H = 32; Dh = D // H
        qkv = (lambda x, *d: x.permute(*d))((x @ wqkv).reshape(B, S, 3, H, Dh), 2, 0, 3, 1, 4)
        q, k, v = qkv[0], qkv[1], qkv[2]
        pq = torch.einsum("bhsd,hdf->bhsf", q, w_phi)
        pk = torch.einsum("bhsd,hdf->bhsf", k, w_phi)
        phi_q = torch.concatenate([_softmax(pq, axis=-1), _softmax(-pq, axis=-1)], axis=-1)
        phi_k = torch.concatenate([_softmax(pk, axis=-1), _softmax(-pk, axis=-1)], axis=-1)
        kv = torch.einsum("bhsf,bhsd->bhfd", phi_k, v)
        z  = torch.einsum("bhsf->bhf", phi_k)
        num = torch.einsum("bhqf,bhfd->bhqd", phi_q, kv)
        den = torch.einsum("bhqf,bhf->bhq", phi_q, z)[..., None] + 1e-6
        o = num / den
        return (lambda x, *d: x.permute(*d))(o, 0, 2, 1, 3).reshape(B, S, D) @ wo

    def _B_ref(x, w1, w2):
        h = torch.einsum("bsd,df->bsf", x, w1)
        zero = torch.zeros_like(h)
        h = torch.maximum(h, zero)
        return torch.einsum("bsf,fd->bsd", h, w2)

    x = A_x
    g = _sigmoid((x * gate_w).sum(-1, keepdims=True))
    a = _A_ref(x, A_wqkv, A_w_phi, A_wo)
    b = _B_ref(x, B_w1, B_w2)
    return g * a + (1 - g) * b
```

```cpp
#include <hip/hip_runtime.h>
#include <hip/hip_bf16.h>
typedef unsigned short u16;
__device__ __forceinline__ float bf2f(u16 b){ return __uint_as_float(((unsigned)b)<<16); }
__device__ __forceinline__ u16 f2bf(float f){ unsigned u=__float_as_uint(f); u += 0x7fffu + ((u>>16)&1u); return (u16)(u>>16); }
constexpr int S=2048, D=512, H=32, DH=16, FF=1792;

__global__ void gemm_naive(const u16* A, const u16* B, float* C, int M, int N, int K, int relu){
  int n = blockIdx.x*blockDim.x + threadIdx.x; int m = blockIdx.y;
  if(n>=N) return;
  float acc=0.f;
  for(int k=0;k<K;k++) acc += bf2f(A[m*K+k])*bf2f(B[k*N+n]);
  if(relu) acc = fmaxf(acc,0.f);
  C[m*N+n]=acc;
}
__global__ void tobf(const float* a, u16* b, int n){ int i=blockIdx.x*blockDim.x+threadIdx.x; if(i<n) b[i]=f2bf(a[i]); }

__global__ void phi_k(const float* qkv, const u16* wphi, float* phi, int which){
  int idx = blockIdx.x*blockDim.x+threadIdx.x;
  int s = idx/H, h = idx%H;
  float p[16];
  for(int f=0;f<16;f++){ float a=0; for(int d=0;d<16;d++) a += bf2f(f2bf(qkv[s*1536+which*512+h*16+d]))*bf2f(wphi[h*256+d*16+f]); p[f]=a; }
  float mx=-1e30f, mn=1e30f; for(int f=0;f<16;f++){ mx=fmaxf(mx,p[f]); mn=fminf(mn,p[f]); }
  float s1=0,s2=0; for(int f=0;f<16;f++){ s1+=__expf(p[f]-mx); s2+=__expf(mn-p[f]); }
  for(int f=0;f<16;f++){ phi[idx*32+f]=__expf(p[f]-mx)/s1; phi[idx*32+16+f]=__expf(mn-p[f])/s2; }
}
__global__ void kv_k(const float* qkv, const float* phik, float* kv){
  int h = blockIdx.x; int t = threadIdx.x;
  int f = t/17, d = t%17;
  float a=0;
  for(int s=0;s<S;s++){ float v = d<16 ? qkv[s*1536+1024+h*16+d] : 1.f; a += phik[(s*H+h)*32+f]*v; }
  kv[h*544+t]=a;
}
__global__ void o_k(const float* phiq, const float* kv, u16* o){
  int idx = blockIdx.x*blockDim.x+threadIdx.x;
  int s = idx/512, h=(idx%512)/16, d=idx%16;
  float num=0, den=0;
  for(int f=0;f<32;f++){ float p=phiq[(s*H+h)*32+f]; num += p*kv[h*544+f*17+d]; den += p*kv[h*544+f*17+16]; }
  o[idx]=f2bf(num/(den+1e-6f));
}
__global__ void fin_k(const u16* x, const u16* gw, const float* a, const float* b, u16* out){
  int s = blockIdx.x; __shared__ float red[512];
  int t=threadIdx.x; red[t]=bf2f(x[s*512+t])*bf2f(gw[t]); __syncthreads();
  for(int o=256;o>0;o>>=1){ if(t<o) red[t]+=red[t+o]; __syncthreads(); }
  float g = 1.f/(1.f+__expf(-red[0]));
  out[s*512+t]=f2bf(g*a[s*512+t]+(1.f-g)*b[s*512+t]);
}
extern "C" void kernel_launch(void* const* d_in, const int* in_sizes, int n_in, void* d_out, int out_size, void* d_ws, size_t ws_size, hipStream_t stream){
  const u16* x=(const u16*)d_in[0]; const u16* wqkv=(const u16*)d_in[1]; const u16* wphi=(const u16*)d_in[2]; const u16* wo=(const u16*)d_in[3];
  const u16* w1=(const u16*)d_in[5]; const u16* w2=(const u16*)d_in[6]; const u16* gw=(const u16*)d_in[7];
  float* ws=(float*)d_ws;
  float* qkv=ws; ws+=S*1536; float* hf=ws; ws+=S*FF; float* phiq=ws; ws+=S*H*32; float* phik=ws; ws+=S*H*32; float* kv=ws; ws+=H*544;
  float* a=ws; ws+=S*D; float* b=ws; ws+=S*D; u16* hb=(u16*)ws; ws+=S*FF/2; u16* ob=(u16*)ws;
  gemm_naive<<<dim3(1536/256,S),256,0,stream>>>(x,wqkv,qkv,S,1536,512,0);
  gemm_naive<<<dim3(FF/256,S),256,0,stream>>>(x,w1,hf,S,FF,512,1);
  tobf<<<S*FF/256,256,0,stream>>>(hf,hb,S*FF);
  phi_k<<<S*H/256,256,0,stream>>>(qkv,wphi,phiq,0);
  phi_k<<<S*H/256,256,0,stream>>>(qkv,wphi,phik,1);
  kv_k<<<H,544,0,stream>>>(qkv,phik,kv);
  o_k<<<S*512/256,256,0,stream>>>(phiq,kv,ob);
  gemm_naive<<<dim3(2,S),256,0,stream>>>(ob,wo,a,S,512,512,0);
  gemm_naive<<<dim3(2,S),256,0,stream>>>(hb,w2,b,S,512,FF,0);
  fin_k<<<S,512,0,stream>>>(x,gw,a,b,(u16*)d_out);
}
```

```cpp
#include <hip/hip_runtime.h>
#include <hip/hip_bf16.h>

typedef unsigned short u16;
typedef unsigned long long u64;
typedef unsigned v4u __attribute__((ext_vector_type(4)));
typedef unsigned v2u __attribute__((ext_vector_type(2)));
typedef short s16x4 __attribute__((ext_vector_type(4)));
typedef short s16x8 __attribute__((ext_vector_type(8)));
typedef __bf16 bf16x8 __attribute__((ext_vector_type(8)));
typedef __bf16 bf16x2 __attribute__((ext_vector_type(2)));
typedef float f32x2 __attribute__((ext_vector_type(2)));
typedef float f32x4 __attribute__((ext_vector_type(4)));
typedef float f32x16 __attribute__((ext_vector_type(16)));
typedef __amdgpu_buffer_rsrc_t rsrc_t;
#define LAS __attribute__((address_space(3)))
#define GAS __attribute__((address_space(1)))
#define DI __device__ __forceinline__
#define RLX_AGENT __ATOMIC_RELAXED, __HIP_MEMORY_SCOPE_AGENT

#define DIAG 0

constexpr unsigned WS_EPOCH = 0;
constexpr unsigned WS_HFLAG = 4096;
constexpr unsigned WS_OFLAG = WS_HFLAG + 128 * 128;
constexpr unsigned WS_KFLAG = WS_OFLAG + 128 * 128;
constexpr unsigned WS_G     = WS_KFLAG + 32 * 128;
constexpr unsigned WS_ZP    = WS_G + 128 * 256;
constexpr unsigned WS_KVP   = WS_ZP + 512 * 128;
constexpr unsigned WS_O     = WS_KVP + 512 * 1024;
constexpr unsigned WS_H     = WS_O + 64 * 32 * 1024;
constexpr unsigned WS_END   = WS_H + 64 * 112 * 1024;
constexpr unsigned WS_DIAG  = WS_END;
constexpr unsigned OOB = 0x7ffffff0u;

constexpr int SLOT = 45056, OFF_WM = 16384, OFF_WS = 40960;
constexpr int LDS_BYTES = 163840;
__host__ __device__ constexpr int slot_base(int i) { return i == 0 ? 0 : (i == 1 ? SLOT : 58 * 2048); }
constexpr int GATE_OFF = OFF_WS;
constexpr int SCR = 36 * 2048;
constexpr int SCR_XCH = SCR;
constexpr int SCR_KS = SCR + 8704;
constexpr int SCR_GX = SCR_KS + 4352;
constexpr int SCR_FLAG = SCR_GX + 512;
constexpr unsigned SPIN_CAP = 1u << 16;

DI void dma(unsigned lds, unsigned voff, rsrc_t r, unsigned soff) {
  asm volatile("s_mov_b32 m0, %0\n\ts_nop 0\n\tbuffer_load_dwordx4 %1, %2, %3 offen lds" ::"s"(lds), "v"(voff), "s"(r), "s"(soff) : "memory");
}
DI s16x8 trfrag(LAS char* p, int off0, int off1) {
  s16x4 a = __builtin_amdgcn_ds_read_tr16_b64_v4i16((LAS s16x4*)(p + off0));
  s16x4 b = __builtin_amdgcn_ds_read_tr16_b64_v4i16((LAS s16x4*)(p + off1));
  return __builtin_shufflevector(a, b, 0, 1, 2, 3, 4, 5, 6, 7);
}
template <class A, class B> DI f32x4 mfma16(A a, B b, f32x4 c) {
  return __builtin_amdgcn_mfma_f32_16x16x32_bf16(__builtin_bit_cast(bf16x8, a), __builtin_bit_cast(bf16x8, b), c, 0, 0, 0);
}
template <class A, class B> DI f32x16 mfma32(A a, B b, f32x16 c) {
  return __builtin_amdgcn_mfma_f32_32x32x16_bf16(__builtin_bit_cast(bf16x8, a), __builtin_bit_cast(bf16x8, b), c, 0, 0, 0);
}
DI unsigned pk(float a, float b) { f32x2 v = {a, b}; return __builtin_bit_cast(unsigned, __builtin_convertvector(v, bf16x2)); }
DI v4u pack8(const f32x16& x, int s) {
  return v4u{pk(x[8 * s], x[8 * s + 1]), pk(x[8 * s + 2], x[8 * s + 3]), pk(x[8 * s + 4], x[8 * s + 5]), pk(x[8 * s + 6], x[8 * s + 7])};
}
DI f32x16 zero16() { f32x16 z; for (int i = 0; i < 16; i++) z[i] = 0.f; return z; }
#define WAITV(n) asm volatile("s_waitcnt vmcnt(" #n ")" ::: "memory")
#define WAITL() asm volatile("s_waitcnt lgkmcnt(0)" ::: "memory")
#define BAR() do { asm volatile("s_waitcnt lgkmcnt(0)" ::: "memory"); __builtin_amdgcn_s_barrier(); asm volatile("" ::: "memory"); } while (0)
#define SCHED0() __builtin_amdgcn_sched_barrier(0)
DI v4u ld_sc1(rsrc_t r, unsigned voff, unsigned soff) { return __builtin_amdgcn_raw_buffer_load_b128(r, voff, soff, 16); }
DI void st_sc1(v4u v, rsrc_t r, unsigned voff, unsigned soff) { __builtin_amdgcn_raw_buffer_store_b128(v, r, voff, soff, 16); }

DI void poll_flags(const GAS u64* p, u64 tag) {
#pragma unroll 1
  for (unsigned spins = 0; spins < SPIN_CAP; ++spins) {
    u64 v = __hip_atomic_load(p, RLX_AGENT);
    if (__all(v == tag)) break;
    __builtin_amdgcn_s_sleep(1);
  }
  __builtin_amdgcn_fence(__ATOMIC_ACQUIRE, "wavefront");
  asm volatile("" ::: "memory");
}
DI void lds_wait(LAS char* L, int idx, u64 tag) {
  volatile LAS u64* f = (volatile LAS u64*)(L + SCR_FLAG + idx * 8);
#pragma unroll 1
  for (unsigned spins = 0; spins < (SPIN_CAP << 4); ++spins) {
    u64 v = *f;
    if (__all(v == tag)) break;
    __builtin_amdgcn_s_sleep(1);
  }
  asm volatile("" ::: "memory");
}
DI void lds_signal(LAS char* L, int idx, u64 tag) {
  asm volatile("s_waitcnt lgkmcnt(0)" ::: "memory");
  *(volatile LAS u64*)(L + SCR_FLAG + idx * 8) = tag;
}
#define STAMP(i) do {} while (0)
#define STAMP_DECL
#define STAMP_FLUSH

DI void softmax16(const f32x16& p, v4u& lo, v4u& hi) {
  float m = fmaxf(fmaxf(p[0], p[1]), p[2]);
#pragma unroll
  for (int i = 3; i < 15; i += 2) m = fmaxf(fmaxf(m, p[i]), p[i + 1]);
  m = fmaxf(m, p[15]);
  const float L2E = 1.4426950408889634f;
  const float mm = -m * L2E;
  f32x16 e; float s0 = 0.f, s1 = 0.f;
#pragma unroll
  for (int i = 0; i < 16; i++) e[i] = __builtin_amdgcn_exp2f(__builtin_fmaf(p[i], L2E, mm));
#pragma unroll
  for (int i = 0; i < 16; i += 2) { s0 += e[i]; s1 += e[i + 1]; }
  const float r = __builtin_amdgcn_rcpf(s0 + s1);
#pragma unroll
  for (int i = 0; i < 16; i++) e[i] *= r;
  lo = pack8(e, 0); hi = pack8(e, 1);
}

__global__ __launch_bounds__(512, 2) void fused_kernel(const u16* __restrict__ x, const u16* __restrict__ wqkv, const u16* __restrict__ wphi,
                                                       const u16* __restrict__ wo, const u16* __restrict__ w1, const u16* __restrict__ w2,
                                                       const u16* __restrict__ gw, u16* __restrict__ out, char* __restrict__ ws) {
  extern __shared__ __attribute__((aligned(16))) char shm[];
  LAS char* L = (LAS char*)shm;
  const unsigned ldsbase = (unsigned)(size_t)L;
  const int tid = threadIdx.x, lane = tid & 63;
  const int wid = __builtin_amdgcn_readfirstlane(tid >> 6);
  const int lw = wid & 3;
  const int b = blockIdx.x, xcd = b & 7, r = b >> 3;
  const bool ffn = (r >> 4) == 0;
  const int ti = (xcd & 1) * 8 + (r & 7), cg = (xcd >> 1) * 2 + ((r >> 3) & 1);
  const int t64 = xcd * 4 + (r & 3), jc = r >> 2;
  const rsrc_t rws = __builtin_amdgcn_make_buffer_rsrc((void*)ws, (short)0, WS_END, 0x00020000);
  STAMP_DECL
  STAMP(0);
  const u64 tag0 = __hip_atomic_load((const GAS u64*)(ws + WS_EPOCH), RLX_AGENT);
  f32x16 acc[2][2];
  const int hh = lane >> 5;
  const int fB = 2 * ((lane >> 3) & 1) + hh;
  const int awB0 = (8 * hh + ((lane & 15) >> 2)) * 128 + ((((lane >> 4) & 1) ^ fB) * 32) + (lane & 3) * 8;
  const int awB1 = awB0 ^ 64;
  const int zb1 = (44 + 3 * lw) * 2048, zb2 = (56 + 6 * lw) * 2048, zb3 = (5 * lw) * 2048, zb4 = (20 + 4 * lw) * 2048;
  const unsigned hflag_off = WS_HFLAG + (((t64 >> 1) * 2 + (t64 & 1)) * 4 + lw) * 128 + (lane & 3) * 8;
  const unsigned hbase = WS_H + (t64 * 2 * 112 + 28 * lw) * 1024;

  if (wid >= 4) {
    const rsrc_t rx = __builtin_amdgcn_make_buffer_rsrc((void*)x, (short)0, 2048 * 512 * 2, 0x00020000);
    const rsrc_t rw = ffn ? __builtin_amdgcn_make_buffer_rsrc((void*)w1, (short)0, 512 * 1792 * 2, 0x00020000)
                          : __builtin_amdgcn_make_buffer_rsrc((void*)wqkv, (short)0, 512 * 1536 * 2, 0x00020000);
    const rsrc_t rw2 = __builtin_amdgcn_make_buffer_rsrc((void*)w2, (short)0, 1792 * 512 * 2, 0x00020000);
    const rsrc_t rwo = __builtin_amdgcn_make_buffer_rsrc((void*)wo, (short)0, 512 * 512 * 2, 0x00020000);
    const int RS = ffn ? 3584 : 3072, LSTR = ffn ? 128 : 1024;
    const int maincol = ffn ? (cg * 448 + (cg & 1) * 64) : cg * 128;
    const int sidecol = cg * 448 + ((cg & 1) ? 0 : 384);
    unsigned vx[2], vw[6], vs, vb[2];
    for (int p = 0; p < 2; p++) vx[p] = (lane >> 3) * 1024 + (((lane & 7) ^ (4 * p + (lane >> 4))) * 16);
    for (int e = 0; e < 6; e++) {
      const int n = 64 * e + lane, row = n / 24, cp = n % 24, f = 2 * ((row >> 1) & 1) + ((row >> 3) & 1);
      vw[e] = row * RS + (cp >> 3) * LSTR + (((cp & 7) ^ (f << 1)) * 16);
    }
    { const int row = lane >> 2; vs = row * 3584 + (((lane & 3) ^ (((row >> 3) & 1) << 1)) * 16); }
    for (int e = 0; e < 2; e++) vb[e] = (8 * e + (lane >> 3)) * 1024 + (((lane & 7) ^ ((2 * ((lane >> 4) & 1) + e) << 1)) * 16);
    auto issue = [&](int s, unsigned sl) {
      if (ffn) dma(sl + OFF_WS + lw * 1024, vs, rw, (s * 64 + 16 * lw) * 3584 + sidecol);
#pragma unroll
      for (int i = 0; i < 4; i++) dma(sl + (4 * lw + i) * 1024, vx[i & 1], rx, ti * 131072 + (4 * lw + i) * 8192 + s * 128);
#pragma unroll
      for (int e = 0; e < 6; e++) dma(sl + OFF_WM + (6 * lw + e) * 1024, vw[e], rw, (s * 64 + 16 * lw) * RS + maincol);
    };
    auto wblk_own = [&](int j, int zoff) {
      const unsigned so = (28 * lw + j) * 16384 + jc * 128;
      dma(ldsbase + zoff, vb[0], rw2, so); dma(ldsbase + zoff + 1024, vb[1], rw2, so);
    };
    auto wblk_c = [&](int j, int zoff) {
      if (j < 10) { const unsigned so = (28 * lw + 18 + j) * 16384 + jc * 128; dma(ldsbase + zoff, vb[0], rw2, so); dma(ldsbase + zoff + 1024, vb[1], rw2, so); }
      else { const unsigned so = (8 * lw + j - 10) * 16384 + jc * 128; dma(ldsbase + zoff, vb[0], rwo, so); dma(ldsbase + zoff + 1024, vb[1], rwo, so); }
    };
    s16x8 wr[18][2];
    auto wread = [&](int j, int zoff) { wr[j][0] = trfrag(L + zoff + awB0, 0, 512); wr[j][1] = trfrag(L + zoff + awB1, 0, 512); };

    issue(0, ldsbase + slot_base(0)); issue(1, ldsbase + slot_base(1));
    WAITV(10); BAR();
    STAMP(1);
    issue(2, ldsbase + slot_base(2));
#pragma unroll
    for (int s = 0; s < 5; s++) { WAITV(10); BAR(); issue(s + 3, ldsbase + slot_base(s % 3)); }
#pragma unroll
    for (int j = 0; j < 3; j++) wblk_own(j, zb1 + j * 2048);
    WAITV(16); BAR();
#pragma unroll
    for (int j = 0; j < 6; j++) wblk_own(3 + j, zb2 + j * 2048);
    WAITV(12);
#pragma unroll
    for (int j = 0; j < 3; j++) wread(j, zb1 + j * 2048);
    WAITL(); SCHED0();
#pragma unroll
    for (int j = 0; j < 3; j++) wblk_c(j, zb1 + j * 2048);
    BAR();
#pragma unroll
    for (int j = 0; j < 5; j++) wblk_own(9 + j, zb3 + j * 2048);
    WAITV(16);
#pragma unroll
    for (int j = 0; j < 6; j++) wread(3 + j, zb2 + j * 2048);
    WAITL(); SCHED0();
#pragma unroll
    for (int j = 0; j < 6; j++) wblk_c(3 + j, zb2 + j * 2048);
    BAR();
    STAMP(2);
#pragma unroll
    for (int j = 0; j < 4; j++) wblk_own(14 + j, zb4 + j * 2048);
    WAITV(20);
#pragma unroll
    for (int j = 0; j < 5; j++) wread(9 + j, zb3 + j * 2048);
    WAITL(); SCHED0();
#pragma unroll
    for (int j = 0; j < 5; j++) wblk_c(9 + j, zb3 + j * 2048);
    WAITV(10);
#pragma unroll
    for (int j = 0; j < 4; j++) wread(14 + j, zb4 + j * 2048);
    WAITL(); SCHED0();
#pragma unroll
    for (int j = 0; j < 4; j++) wblk_c(14 + j, zb4 + j * 2048);
    const u64 tag = (((u64)__builtin_amdgcn_readfirstlane((unsigned)(tag0 >> 32)) << 32) | __builtin_amdgcn_readfirstlane((unsigned)tag0)) + 1;
    WAITV(0);
    lds_signal(L, 8 + lw, tag);
    STAMP(3);
    if (!ffn) {
      const int hd = 4 * cg + lw;
      poll_flags((const GAS u64*)(ws + WS_KFLAG + hd * 128 + (lane & 15) * 8), tag);
      STAMP(4);
      v4u kp[16];
#pragma unroll
      for (int i = 0; i < 16; i++) kp[i] = ld_sc1(rws, lane * 16, WS_KVP + (hd * 16 + i) * 1024);
      const v4u zp = ld_sc1(rws, (lane & 15) * 128 + (lane >> 4) * 16, WS_ZP + hd * 2048);
      const int q = lane >> 4, rho = lane & 15;
      v4u sel[2];
#pragma unroll
      for (int u = 0; u < 2; u++)
#pragma unroll
        for (int jj = 0; jj < 4; jj++) {
          const int f0 = 16 * (q >> 1) + 8 * (jj >> 1) + 4 * (q & 1) + 2 * (jj & 1);
          sel[u][jj] = ((f0 == 16 * u + rho) ? 0x3f80u : 0u) | ((f0 + 1 == 16 * u + rho) ? 0x3f800000u : 0u);
        }
      f32x4 t0 = {0, 0, 0, 0}, t1 = {0, 0, 0, 0};
#pragma unroll
      for (int i = 0; i < 16; i++) { t0 = mfma16(sel[0], kp[i], t0); t1 = mfma16(sel[1], kp[i], t1); }
      LAS char* ks = L + SCR_KS + lw * 1088;
      *(LAS v2u*)(ks + rho * 64 + q * 8) = v2u{pk(t0[0], t0[1]), pk(t0[2], t0[3])};
      *(LAS v2u*)(ks + rho * 64 + 32 + q * 8) = v2u{pk(t1[0], t1[1]), pk(t1[2], t1[3])};
      float z[8];
#pragma unroll
      for (int jj = 0; jj < 4; jj++) { z[2 * jj] = __uint_as_float(zp[jj] << 16); z[2 * jj + 1] = __uint_as_float(zp[jj] & 0xffff0000u); }
#pragma unroll
      for (int jj = 0; jj < 8; jj++) {
        z[jj] += __uint_as_float(__builtin_amdgcn_update_dpp(0u, __float_as_uint(z[jj]), 0x128, 0xf, 0xf, false));
        z[jj] += __uint_as_float(__builtin_amdgcn_update_dpp(0u, __float_as_uint(z[jj]), 0x124, 0xf, 0xf, false));
        z[jj] += __uint_as_float(__builtin_amdgcn_update_dpp(0u, __float_as_uint(z[jj]), 0x122, 0xf, 0xf, false));
        z[jj] += __uint_as_float(__builtin_amdgcn_update_dpp(0u, __float_as_uint(z[jj]), 0x121, 0xf, 0xf, false));
      }
      if (rho == 0) {
        LAS char* zr = ks + 1024 + (16 * (q >> 1) + 4 * (q & 1)) * 2;
        *(LAS v2u*)(zr) = v2u{pk(z[0], z[1]), pk(z[2], z[3])};
        *(LAS v2u*)(zr + 16) = v2u{pk(z[4], z[5]), pk(z[6], z[7])};
      }
      lds_signal(L, 4 + lw, tag);
    }
    STAMP(5);
    poll_flags((const GAS u64*)(ws + hflag_off), tag);
    STAMP(6);
    v4u av[18][2];
    constexpr int W0 = 4;
#pragma unroll
    for (int j = 0; j < W0; j++) { av[j][0] = ld_sc1(rws, lane * 16, hbase + j * 1024); av[j][1] = ld_sc1(rws, lane * 16, hbase + (112 + j) * 1024); }
    SCHED0();
    acc[0][0] = zero16(); acc[0][1] = zero16(); acc[1][0] = zero16(); acc[1][1] = zero16();
#pragma unroll
    for (int j = 0; j < 18; j++) {
      acc[0][0] = mfma32(wr[j][0], av[j][0], acc[0][0]);
      acc[1][0] = mfma32(wr[j][1], av[j][0], acc[1][0]);
      acc[0][1] = mfma32(wr[j][0], av[j][1], acc[0][1]);
      acc[1][1] = mfma32(wr[j][1], av[j][1], acc[1][1]);
      SCHED0();
#pragma unroll
      for (int n = W0 + 2 * j; n < W0 + 2 * j + 2; n++)
        if (n < 18) { av[n][0] = ld_sc1(rws, lane * 16, hbase + n * 1024); av[n][1] = ld_sc1(rws, lane * 16, hbase + (112 + n) * 1024); }
      SCHED0();
    }
    STAMP(7);
    poll_flags((const GAS u64*)(ws + hflag_off + (WS_OFLAG - WS_HFLAG)), tag);
    {
      const unsigned go = WS_G + (t64 * 4 + lw) * 256;
      const float g0 = __uint_as_float(__builtin_amdgcn_raw_buffer_load_b32(rws, (lane & 31) * 4, go, 16));
      const float g1 = __uint_as_float(__builtin_amdgcn_raw_buffer_load_b32(rws, (lane & 31) * 4, go + 128, 16));
#pragma unroll
      for (int i = 0; i < 16; i++) { acc[0][0][i] *= g0; acc[1][0][i] *= g0; acc[0][1][i] *= g1; acc[1][1][i] *= g1; }
    }
    if (b == 0 && lw == 3) {
      const int w0 = lane, w1i = lane + 64;
      poll_flags((const GAS u64*)(ws + WS_HFLAG + ((w0 >> 3) * 8 + ((w0 & 7) >> 1)) * 128 + (w0 & 1) * 16), tag);
      poll_flags((const GAS u64*)(ws + WS_HFLAG + ((w1i >> 3) * 8 + ((w1i & 7) >> 1)) * 128 + (w1i & 1) * 16), tag);
      poll_flags((const GAS u64*)(ws + WS_KFLAG + (w0 >> 4) * 4 * 128 + (w0 & 15) * 8), tag);
      poll_flags((const GAS u64*)(ws + WS_KFLAG + (w1i >> 4) * 4 * 128 + (w1i & 15) * 8), tag);
      if (lane == 0) __hip_atomic_store((GAS u64*)(ws + WS_EPOCH), tag, RLX_AGENT);
    }
  } else {
    const int m = lw & 1, nh = lw >> 1, q = lane >> 4;
    u64 tag;
    if (ffn) {
      int ax0 = m * 8192 + (lane & 15) * 128 + ((q ^ ((lane & 15) >> 1)) * 16);
      int aw[6], as_;
      { const int f = 2 * ((lane >> 3) & 1) + (q & 1), rb = 8 * q + ((lane & 15) >> 2);
        for (int i = 0; i < 6; i++) { const int T = 6 * nh + i; aw[i] = OFF_WM + rb * 384 + (lane & 3) * 8 + (((T & 3) ^ f) * 32) + (T >> 2) * 128; }
        as_ = OFF_WS + rb * 64 + ((nh ^ (q & 1)) * 32) + (lane & 3) * 8; }
      f32x4 ca[7][4];
      for (int i = 0; i < 7; i++) for (int j = 0; j < 4; j++) ca[i][j] = f32x4{0, 0, 0, 0};
      s16x8 xf[2][4], wf[2][7];
      auto rd = [&](int sb, int u, int bi) {
        LAS char* S = L + sb;
#pragma unroll
        for (int t = 0; t < 4; t++) xf[bi][t] = *(LAS s16x8*)(S + (ax0 ^ (u * 64)) + t * 2048);
#pragma unroll
        for (int i = 0; i < 6; i++) wf[bi][i] = trfrag(S + aw[i], (32 * u) * 384, (32 * u + 4) * 384);
        wf[bi][6] = trfrag(S + as_, (32 * u) * 64, (32 * u + 4) * 64);
      };
      auto mm = [&](int bi) {
#pragma unroll
        for (int i = 0; i < 7; i++)
#pragma unroll
          for (int t = 0; t < 4; t++) ca[i][t] = mfma16(wf[bi][i], xf[bi][t], ca[i][t]);
#pragma unroll
        for (int i = 0; i < 18; i++) { __builtin_amdgcn_sched_group_barrier(0x8, 1, 0); __builtin_amdgcn_sched_group_barrier(0x100, 1, 0); }
        __builtin_amdgcn_sched_group_barrier(0x8, 10, 0);
      };
      tag = (((u64)__builtin_amdgcn_readfirstlane((unsigned)(tag0 >> 32)) << 32) | __builtin_amdgcn_readfirstlane((unsigned)tag0)) + 1;
      WAITV(0);
      BAR();
      STAMP(1);
      int sb = slot_base(0);
      rd(sb, 0, 0);
#pragma unroll 1
      for (int s = 0; s < 8; s++) {
        rd(sb, 1, 1); mm(0); SCHED0();
        BAR();
        sb = (sb == slot_base(0)) ? slot_base(1) : (sb == slot_base(1) ? slot_base(2) : slot_base(0));
        rd(sb, 0, 0); mm(1); SCHED0();
      }
      STAMP(2);
      const int kt_main = 14 * cg + ((cg & 1) ? 2 : 0) + 6 * nh, kt_side = 14 * cg + ((cg & 1) ? 0 : 12) + nh;
#pragma unroll
      for (int i = 0; i < 7; i++) {
        const int kt = (i < 6) ? kt_main + i : kt_side;
#pragma unroll
        for (int a = 0; a < 2; a++) {
          const f32x4 c0 = ca[i][2 * a], c1 = ca[i][2 * a + 1];
          const unsigned x0 = pk(fmaxf(c0[0], 0.f), fmaxf(c0[1], 0.f)), x1 = pk(fmaxf(c0[2], 0.f), fmaxf(c0[3], 0.f));
          const unsigned y0 = pk(fmaxf(c1[0], 0.f), fmaxf(c1[1], 0.f)), y1 = pk(fmaxf(c1[2], 0.f), fmaxf(c1[3], 0.f));
          const auto s0 = __builtin_amdgcn_permlane16_swap(x0, y0, false, false);
          const auto s1 = __builtin_amdgcn_permlane16_swap(x1, y1, false, false);
          st_sc1(v4u{s0[0], s1[0], s0[1], s1[1]}, rws, lane * 16, WS_H + ((ti * 4 + m * 2 + a) * 112 + kt) * 1024);
        }
      }
      STAMP(3);
      WAITV(0);
      if (lane == 0) __hip_atomic_store((GAS u64*)(ws + WS_HFLAG + ((ti * 2 + m) * 4 + (cg >> 1)) * 128 + ((cg & 1) * 2 + nh) * 8), tag, RLX_AGENT);
      STAMP(4);
    } else {
      const int p = nh;
      if (lw == 0) { *(LAS v4u*)(L + GATE_OFF + lane * 16) = *(const GAS v4u*)((const GAS char*)gw + lane * 16); }
      if (lw == 1) { *(LAS v4u*)(L + GATE_OFF + 1024 + lane * 16) = v4u{0, 0, 0, 0}; }
      v4u wp[2];
      { const int rho = lane & 31, f = (rho & 3) + 4 * (rho >> 3); const unsigned sg = ((rho >> 2) & 1) ? 0x80008000u : 0u;
#pragma unroll
        for (int s = 0; s < 2; s++) {
          const GAS u16* w = (const GAS u16*)wphi + (4 * cg + 2 * p + s) * 256 + (4 * hh) * 16 + f;
#pragma unroll
          for (int jj = 0; jj < 4; jj++) { const int d = 8 * (jj >> 1) + 2 * (jj & 1); wp[s][jj] = (((unsigned)w[d * 16]) | (((unsigned)w[(d + 1) * 16]) << 16)) ^ sg; }
        } }
      const int ax0 = m * 8192 + (lane & 31) * 128 + ((hh ^ (((lane & 31) >> 1) & 7)) * 16);
      const int awq = OFF_WM + (8 * hh + ((lane & 15) >> 2)) * 384 + (((2 * p + ((lane >> 4) & 1)) ^ fB) * 32) + (lane & 3) * 8;
      const int ag = GATE_OFF + (((q & 1) == ((lane >> 2) & 1)) ? 0 : 1024) + (q >> 1) * 16;
      f32x16 cq[2], ck[2], cv[2]; f32x4 cgate = {0, 0, 0, 0};
      for (int t = 0; t < 2; t++) { cq[t] = zero16(); ck[t] = zero16(); cv[t] = zero16(); }
      s16x8 xf[2][2][2], wf[2][2][3], gf[2][2];
      auto rd = [&](int sb, int goff, int u, int bi) {
        LAS char* S = L + sb;
#pragma unroll
        for (int k = 0; k < 2; k++) {
          const int s16 = 2 * u + k;
#pragma unroll
          for (int t = 0; t < 2; t++) xf[bi][k][t] = *(LAS s16x8*)(S + (ax0 ^ (s16 * 32)) + t * 4096);
#pragma unroll
          for (int i = 0; i < 3; i++) wf[bi][k][i] = trfrag(S + awq, (16 * s16) * 384 + i * 128, (16 * s16 + 4) * 384 + i * 128);
          gf[bi][k] = *(LAS s16x8*)(L + ag + goff + s16 * 32);
        }
      };
      auto mm = [&](int bi) {
#pragma unroll
        for (int k = 0; k < 2; k++) {
#pragma unroll
          for (int t = 0; t < 2; t++) {
            ck[t] = mfma32(wf[bi][k][1], xf[bi][k][t], ck[t]);
            cv[t] = mfma32(xf[bi][k][t], wf[bi][k][2], cv[t]);
            cq[t] = mfma32(wf[bi][k][0], xf[bi][k][t], cq[t]);
          }
          cgate = mfma16(gf[bi][k], p ? xf[bi][k][1] : xf[bi][k][0], cgate);
        }
#pragma unroll
        for (int i = 0; i < 9; i++) { __builtin_amdgcn_sched_group_barrier(0x8, 1, 0); __builtin_amdgcn_sched_group_barrier(0x100, 2, 0); }
        __builtin_amdgcn_sched_group_barrier(0x8, 5, 0);
      };
      tag = (((u64)__builtin_amdgcn_readfirstlane((unsigned)(tag0 >> 32)) << 32) | __builtin_amdgcn_readfirstlane((unsigned)tag0)) + 1;
      WAITV(0);
      BAR();
      STAMP(1);
      int sb = slot_base(0);
      rd(sb, 0, 0, 0);
#pragma unroll 1
      for (int s = 0; s < 8; s++) {
        rd(sb, s * 128, 1, 1); mm(0); SCHED0();
        BAR();
        sb = (sb == slot_base(0)) ? slot_base(1) : (sb == slot_base(1) ? slot_base(2) : slot_base(0));
        rd(sb, ((s + 1) & 7) * 128, 0, 0); mm(1); SCHED0();
      }
      STAMP(2);
      v4u idn[2];
#pragma unroll
      for (int s = 0; s < 2; s++)
#pragma unroll
        for (int jj = 0; jj < 4; jj++) { const int F = 16 * hh + 8 * s + 2 * jj, c = lane & 31; idn[s][jj] = ((c == F) ? 0x3f80u : 0u) | ((c == F + 1) ? 0x3f800000u : 0u); }
      f32x16 kv[2] = {zero16(), zero16()};
      const bool upper = (lane & 16) != 0;
#pragma unroll
      for (int t = 0; t < 2; t++) {
        const v4u v0 = pack8(cv[t], 0), v1 = pack8(cv[t], 1);
#pragma unroll
        for (int s = 0; s < 2; s++) {
          const f32x16 pkk = mfma32(wp[s], pack8(ck[t], s), zero16());
          v4u lo, hi; softmax16(pkk, lo, hi);
          f32x16 z = mfma32(lo, idn[0], zero16());
          z = mfma32(hi, idn[1], z);
          v4u e0, e1;
#pragma unroll
          for (int jj = 0; jj < 4; jj++) { e0[jj] = (upper == (s == 0)) ? 0x3f803f80u : v0[jj]; e1[jj] = (upper == (s == 0)) ? 0x3f803f80u : v1[jj]; }
          kv[s] = mfma32(pack8(z, 0), e0, kv[s]);
          kv[s] = mfma32(pack8(z, 1), e1, kv[s]);
        }
      }
      {
        const bool upo = (m == 0);
        const bool data_o = (upper == upo), zl_o = !data_o && (lane & 15) == 0;
        const f32x16 o = m ? kv[0] : kv[1];
        if (data_o || zl_o) {
          LAS f32x4* d = (LAS f32x4*)(L + SCR_XCH + lw * 2176 + (hh * 17 + (data_o ? (lane & 15) : 16)) * 64);
#pragma unroll
          for (int i = 0; i < 4; i++) d[i] = f32x4{o[4 * i], o[4 * i + 1], o[4 * i + 2], o[4 * i + 3]};
        }
        if (lane < 32) *(LAS float*)(L + SCR_GX + lw * 128 + lane * 4) = cgate[0];
        lds_signal(L, lw, tag);
        lds_wait(L, lw ^ 1, tag);
        const bool data_m = (upper == (m == 1)), zl_m = !data_m && (lane & 15) == 0;
        f32x16 own = m ? kv[1] : kv[0];
        const LAS f32x4* sp = (const LAS f32x4*)(L + SCR_XCH + (lw ^ 1) * 2176 + (hh * 17 + (data_m ? (lane & 15) : 16)) * 64);
#pragma unroll
        for (int i = 0; i < 4; i++) { const f32x4 v = sp[i]; own[4 * i] += v[0]; own[4 * i + 1] += v[1]; own[4 * i + 2] += v[2]; own[4 * i + 3] += v[3]; }
        const int hd = 4 * cg + 2 * p + m;
        const unsigned o0 = data_m ? (WS_KVP + (hd * 16 + ti) * 1024 + (hh * 16 + (lane & 15)) * 16) : (zl_m ? (WS_ZP + (hd * 16 + ti) * 128 + hh * 16) : OOB);
        const unsigned o1 = data_m ? o0 + 512 : (zl_m ? o0 + 32 : OOB);
        st_sc1(pack8(own, 0), rws, o0, 0);
        st_sc1(pack8(own, 1), rws, o1, 0);
        STAMP(3);
        WAITV(0);
        if (lane == 0) __hip_atomic_store((GAS u64*)(ws + WS_KFLAG + hd * 128 + ti * 8), tag, RLX_AGENT);
      }
      STAMP(4);
      v4u pq[2][2][2];
#pragma unroll
      for (int t = 0; t < 2; t++)
#pragma unroll
        for (int s = 0; s < 2; s++) { const f32x16 pp = mfma32(wp[s], pack8(cq[t], s), zero16()); softmax16(pp, pq[t][s][0], pq[t][s][1]);
          asm volatile("" : "+v"(pq[t][s][0]), "+v"(pq[t][s][1])); }
      lds_wait(L, lw ^ 2, tag);
      float gt[2];
      { const float other = *(const LAS float*)(L + SCR_GX + (lw ^ 2) * 128 + (lane & 31) * 4);
        const float l0 = p ? other : cgate[0], l1 = p ? cgate[0] : other;
        gt[0] = __builtin_amdgcn_rcpf(1.f + __builtin_amdgcn_exp2f(-1.4426950408889634f * l0));
        gt[1] = __builtin_amdgcn_rcpf(1.f + __builtin_amdgcn_exp2f(-1.4426950408889634f * l1)); }
      STAMP(5);
      { const int rho = lane & 31, krow = rho < 16 ? (8 * ((rho >> 2) & 1) + (rho & 3) + 4 * (rho >> 3)) : 16;
#pragma unroll
        for (int s = 0; s < 2; s++) {
          lds_wait(L, 4 + 2 * p + s, tag);
          const LAS char* ks = L + SCR_KS + (2 * p + s) * 1088 + krow * 64 + hh * 32;
          const v4u k0 = *(const LAS v4u*)ks, k1 = *(const LAS v4u*)(ks + 16);
#pragma unroll
          for (int t = 0; t < 2; t++) {
            f32x16 o = mfma32(k0, pq[t][s][0], zero16());
            o = mfma32(k1, pq[t][s][1], o);
            const float sc = gt[t] * __builtin_amdgcn_rcpf(o[8] + 1e-6f);
#pragma unroll
            for (int i = 0; i < 8; i++) o[i] *= sc;
            st_sc1(pack8(o, 0), rws, lane * 16, WS_O + ((ti * 4 + m * 2 + t) * 32 + 4 * cg + 2 * p + s) * 1024);
          }
        }
        const unsigned go = ((cg & 1) == 0 && p == 0) ? (WS_G + ((ti * 2 + m) * 4 + (cg >> 1)) * 256 + lane * 4) : OOB;
        __builtin_amdgcn_raw_buffer_store_b32(__float_as_uint(1.f - (hh ? gt[1] : gt[0])), rws, go, 0, 16);
        WAITV(0);
        if (lane == 0) __hip_atomic_store((GAS u64*)(ws + WS_OFLAG + ((ti * 2 + m) * 4 + (cg >> 1)) * 128 + ((cg & 1) * 2 + p) * 8), tag, RLX_AGENT);
      }
    }
    STAMP(6);
    poll_flags((const GAS u64*)(ws + hflag_off), tag);
    STAMP(7);
    v4u av[10][2];
#pragma unroll
    for (int j = 0; j < 10; j++) { av[j][0] = ld_sc1(rws, lane * 16, hbase + (18 + j) * 1024); av[j][1] = ld_sc1(rws, lane * 16, hbase + (112 + 18 + j) * 1024); }
    SCHED0();
    lds_wait(L, 8 + lw, tag);
    STAMP(8);
    acc[0][0] = zero16(); acc[0][1] = zero16(); acc[1][0] = zero16(); acc[1][1] = zero16();
    auto zoff = [&](int j) { return j < 3 ? zb1 + j * 2048 : (j < 9 ? zb2 + (j - 3) * 2048 : (j < 14 ? zb3 + (j - 9) * 2048 : zb4 + (j - 14) * 2048)); };
#pragma unroll
    for (int j = 0; j < 10; j++) {
      const s16x8 w0 = trfrag(L + zoff(j) + awB0, 0, 512), w1f = trfrag(L + zoff(j) + awB1, 0, 512);
      acc[0][0] = mfma32(w0, av[j][0], acc[0][0]);
      acc[1][0] = mfma32(w1f, av[j][0], acc[1][0]);
      acc[0][1] = mfma32(w0, av[j][1], acc[0][1]);
      acc[1][1] = mfma32(w1f, av[j][1], acc[1][1]);
    }
    SCHED0();
    STAMP(9);
    poll_flags((const GAS u64*)(ws + hflag_off + (WS_OFLAG - WS_HFLAG)), tag);
    STAMP(10);
    v4u ov[8][2];
    const unsigned go = WS_G + (t64 * 4 + lw) * 256;
    const float g0 = __uint_as_float(__builtin_amdgcn_raw_buffer_load_b32(rws, (lane & 31) * 4, go, 16));
    const float g1 = __uint_as_float(__builtin_amdgcn_raw_buffer_load_b32(rws, (lane & 31) * 4, go + 128, 16));
    const unsigned obase = WS_O + (t64 * 2 * 32 + 8 * lw) * 1024;
#pragma unroll
    for (int j = 0; j < 8; j++) { ov[j][0] = ld_sc1(rws, lane * 16, obase + j * 1024); ov[j][1] = ld_sc1(rws, lane * 16, obase + (32 + j) * 1024); }
    SCHED0();
#pragma unroll
    for (int i = 0; i < 16; i++) { acc[0][0][i] *= g0; acc[1][0][i] *= g0; acc[0][1][i] *= g1; acc[1][1][i] *= g1; }
#pragma unroll
    for (int j = 0; j < 8; j++) {
      const s16x8 w0 = trfrag(L + zoff(10 + j) + awB0, 0, 512), w1f = trfrag(L + zoff(10 + j) + awB1, 0, 512);
      acc[0][0] = mfma32(w0, ov[j][0], acc[0][0]);
      acc[1][0] = mfma32(w1f, ov[j][0], acc[1][0]);
      acc[0][1] = mfma32(w0, ov[j][1], acc[0][1]);
      acc[1][1] = mfma32(w1f, ov[j][1], acc[1][1]);
    }
  }
  STAMP(11);
  BAR();
  {
    LAS char* wbase = L + wid * 2048 + lane * 16;
#pragma unroll
    for (int d = 0; d < 8; d++) {
      const f32x16& t = acc[d >> 2][(d >> 1) & 1];
      const int o = 8 * (d & 1);
      if (d != wid) {
        *(LAS f32x4*)(wbase + d * 16384) = f32x4{t[o], t[o + 1], t[o + 2], t[o + 3]};
        *(LAS f32x4*)(wbase + d * 16384 + 1024) = f32x4{t[o + 4], t[o + 5], t[o + 6], t[o + 7]};
      }
    }
    f32x4 s0, s1;
    {
      const f32x16 t = (wid & 4) ? ((wid & 2) ? acc[1][1] : acc[1][0]) : ((wid & 2) ? acc[0][1] : acc[0][0]);
      s0 = (wid & 1) ? f32x4{t[8], t[9], t[10], t[11]} : f32x4{t[0], t[1], t[2], t[3]};
      s1 = (wid & 1) ? f32x4{t[12], t[13], t[14], t[15]} : f32x4{t[4], t[5], t[6], t[7]};
    }
    BAR();
    STAMP(12);
    const LAS char* rbase = L + wid * 16384 + lane * 16;
#pragma unroll
    for (int sidx = 1; sidx < 8; sidx++) {
      const int src = (wid + sidx) & 7;
      s0 += *(const LAS f32x4*)(rbase + src * 2048);
      s1 += *(const LAS f32x4*)(rbase + src * 2048 + 1024);
    }
    const unsigned a0 = pk(s0[0], s0[1]), a1 = pk(s0[2], s0[3]), b0 = pk(s1[0], s1[1]), b1 = pk(s1[2], s1[3]);
    const auto r0 = __builtin_amdgcn_permlane32_swap(a0, b0, false, false);
    const auto r1 = __builtin_amdgcn_permlane32_swap(a1, b1, false, false);
    const int tok = t64 * 64 + ((wid >> 1) & 1) * 32 + (lane & 31);
    const int col = jc * 64 + (wid >> 2) * 32 + (wid & 1) * 16 + hh * 8;
    *(GAS v4u*)((GAS u16*)out + tok * 512 + col) = v4u{r0[0], r1[0], r0[1], r1[1]};
  }
  STAMP(13);
  STAMP_FLUSH;
}

extern "C" void kernel_launch(void* const* d_in, const int* in_sizes, int n_in, void* d_out, int out_size, void* d_ws, size_t ws_size,
                              hipStream_t stream) {
  (void)hipFuncSetAttribute((const void*)fused_kernel, hipFuncAttributeMaxDynamicSharedMemorySize, LDS_BYTES);
  fused_kernel<<<256, 512, LDS_BYTES, stream>>>((const u16*)d_in[0], (const u16*)d_in[1], (const u16*)d_in[2], (const u16*)d_in[3],
                                                (const u16*)d_in[5], (const u16*)d_in[6], (const u16*)d_in[7], (u16*)d_out, (char*)d_ws);
}
```

```cpp
#include <hip/hip_runtime.h>
#include <hip/hip_bf16.h>

typedef unsigned short u16;
typedef unsigned long long u64;
typedef unsigned v4u __attribute__((ext_vector_type(4)));
typedef unsigned v2u __attribute__((ext_vector_type(2)));
typedef short s16x4 __attribute__((ext_vector_type(4)));
typedef short s16x8 __attribute__((ext_vector_type(8)));
typedef __bf16 bf16x8 __attribute__((ext_vector_type(8)));
typedef __bf16 bf16x2 __attribute__((ext_vector_type(2)));
typedef float f32x2 __attribute__((ext_vector_type(2)));
typedef float f32x4 __attribute__((ext_vector_type(4)));
typedef float f32x16 __attribute__((ext_vector_type(16)));
typedef __amdgpu_buffer_rsrc_t rsrc_t;
#define LAS __attribute__((address_space(3)))
#define GAS __attribute__((address_space(1)))
#define DI __device__ __forceinline__
#define RLX_AGENT __ATOMIC_RELAXED, __HIP_MEMORY_SCOPE_AGENT

#define DIAG 2

constexpr unsigned WS_EPOCH = 0;
constexpr unsigned WS_HFLAG = 4096;
constexpr unsigned WS_OFLAG = WS_HFLAG + 128 * 128;
constexpr unsigned WS_KFLAG = WS_OFLAG + 128 * 128;
constexpr unsigned WS_G     = WS_KFLAG + 32 * 128;
constexpr unsigned WS_ZP    = WS_G + 128 * 256;
constexpr unsigned WS_KVP   = WS_ZP + 512 * 128;
constexpr unsigned WS_O     = WS_KVP + 512 * 1024;
constexpr unsigned WS_H     = WS_O + 64 * 32 * 1024;
constexpr unsigned WS_END   = WS_H + 64 * 112 * 1024;
constexpr unsigned WS_DIAG  = WS_END;
constexpr unsigned OOB = 0x7ffffff0u;

constexpr int SLOT = 45056, OFF_WM = 16384, OFF_WS = 40960;
constexpr int LDS_BYTES = 163840;
__host__ __device__ constexpr int slot_base(int i) { return i == 0 ? 0 : (i == 1 ? SLOT : 58 * 2048); }
constexpr int GATE_OFF = OFF_WS;
constexpr int SCR = 36 * 2048;
constexpr int SCR_XCH = SCR;
constexpr int SCR_KS = SCR + 8704;
constexpr int SCR_GX = SCR_KS + 4352;
constexpr int SCR_FLAG = SCR_GX + 512;
constexpr unsigned SPIN_CAP = 1u << 16;

DI void dma(unsigned lds, unsigned voff, rsrc_t r, unsigned soff) {
  asm volatile("s_mov_b32 m0, %0\n\ts_nop 0\n\tbuffer_load_dwordx4 %1, %2, %3 offen lds" ::"s"(lds), "v"(voff), "s"(r), "s"(soff) : "memory");
}
DI s16x8 trfrag(LAS char* p, int off0, int off1) {
  s16x4 a = __builtin_amdgcn_ds_read_tr16_b64_v4i16((LAS s16x4*)(p + off0));
  s16x4 b = __builtin_amdgcn_ds_read_tr16_b64_v4i16((LAS s16x4*)(p + off1));
  return __builtin_shufflevector(a, b, 0, 1, 2, 3, 4, 5, 6, 7);
}
template <class A, class B> DI f32x4 mfma16(A a, B b, f32x4 c) {
  return __builtin_amdgcn_mfma_f32_16x16x32_bf16(__builtin_bit_cast(bf16x8, a), __builtin_bit_cast(bf16x8, b), c, 0, 0, 0);
}
template <class A, class B> DI f32x16 mfma32(A a, B b, f32x16 c) {
  return __builtin_amdgcn_mfma_f32_32x32x16_bf16(__builtin_bit_cast(bf16x8, a), __builtin_bit_cast(bf16x8, b), c, 0, 0, 0);
}
DI unsigned pk(float a, float b) { f32x2 v = {a, b}; return __builtin_bit_cast(unsigned, __builtin_convertvector(v, bf16x2)); }
DI v4u pack8(const f32x16& x, int s) {
  return v4u{pk(x[8 * s], x[8 * s + 1]), pk(x[8 * s + 2], x[8 * s + 3]), pk(x[8 * s + 4], x[8 * s + 5]), pk(x[8 * s + 6], x[8 * s + 7])};
}
DI f32x16 zero16() { f32x16 z; for (int i = 0; i < 16; i++) z[i] = 0.f; return z; }
#define WAITV(n) asm volatile("s_waitcnt vmcnt(" #n ")" ::: "memory")
#define WAITL() asm volatile("s_waitcnt lgkmcnt(0)" ::: "memory")
#define BAR() do { asm volatile("s_waitcnt lgkmcnt(0)" ::: "memory"); __builtin_amdgcn_s_barrier(); asm volatile("" ::: "memory"); } while (0)
#define SCHED0() __builtin_amdgcn_sched_barrier(0)
DI v4u ld_sc1(rsrc_t r, unsigned voff, unsigned soff) { return __builtin_amdgcn_raw_buffer_load_b128(r, voff, soff, 16); }
DI void st_sc1(v4u v, rsrc_t r, unsigned voff, unsigned soff) { __builtin_amdgcn_raw_buffer_store_b128(v, r, voff, soff, 16); }

DI void poll_flags(const GAS u64* p, u64 tag) {
#pragma unroll 1
  for (unsigned spins = 0; spins < SPIN_CAP; ++spins) {
    u64 v = __hip_atomic_load(p, RLX_AGENT);
    if (__all(v == tag)) break;
    __builtin_amdgcn_s_sleep(1);
  }
  __builtin_amdgcn_fence(__ATOMIC_ACQUIRE, "wavefront");
  asm volatile("" ::: "memory");
}
DI void lds_wait(LAS char* L, int idx, u64 tag) {
  volatile LAS u64* f = (volatile LAS u64*)(L + SCR_FLAG + idx * 8);
#pragma unroll 1
  for (unsigned spins = 0; spins < (SPIN_CAP << 4); ++spins) {
    u64 v = *f;
    if (__all(v == tag)) break;
    __builtin_amdgcn_s_sleep(1);
  }
  asm volatile("" ::: "memory");
}
DI void lds_signal(LAS char* L, int idx, u64 tag) {
  asm volatile("s_waitcnt lgkmcnt(0)" ::: "memory");
  *(volatile LAS u64*)(L + SCR_FLAG + idx * 8) = tag;
}
#define STAMP(i) do { SCHED0(); stamps[i] = (unsigned)__builtin_amdgcn_s_memrealtime(); __builtin_amdgcn_s_waitcnt(0xC07F); SCHED0(); } while (0)
#define STAMP_DECL unsigned stamps[16]; for (int i_ = 0; i_ < 16; i_++) stamps[i_] = 0; stamps[14] = (unsigned)__builtin_amdgcn_s_memtime();
#define STAMP_FLUSH do { stamps[15] = (unsigned)__builtin_amdgcn_s_memtime(); if ((threadIdx.x & 63) == 0) { GAS unsigned* d_ = (GAS unsigned*)(ws + WS_DIAG) + (blockIdx.x * 8 + (threadIdx.x >> 6)) * 16; \
  for (int i_ = 0; i_ < 16; i_++) d_[i_] = stamps[i_]; } } while (0)

DI void softmax16(const f32x16& p, v4u& lo, v4u& hi) {
  float m = fmaxf(fmaxf(p[0], p[1]), p[2]);
#pragma unroll
  for (int i = 3; i < 15; i += 2) m = fmaxf(fmaxf(m, p[i]), p[i + 1]);
  m = fmaxf(m, p[15]);
  const float L2E = 1.4426950408889634f;
  const float mm = -m * L2E;
  f32x16 e; float s0 = 0.f, s1 = 0.f;
#pragma unroll
  for (int i = 0; i < 16; i++) e[i] = __builtin_amdgcn_exp2f(__builtin_fmaf(p[i], L2E, mm));
#pragma unroll
  for (int i = 0; i < 16; i += 2) { s0 += e[i]; s1 += e[i + 1]; }
  const float r = __builtin_amdgcn_rcpf(s0 + s1);
#pragma unroll
  for (int i = 0; i < 16; i++) e[i] *= r;
  lo = pack8(e, 0); hi = pack8(e, 1);
}

__global__ __launch_bounds__(512, 2) void fused_kernel(const u16* __restrict__ x, const u16* __restrict__ wqkv, const u16* __restrict__ wphi,
                                                       const u16* __restrict__ wo, const u16* __restrict__ w1, const u16* __restrict__ w2,
                                                       const u16* __restrict__ gw, u16* __restrict__ out, char* __restrict__ ws) {
  extern __shared__ __attribute__((aligned(16))) char shm[];
  LAS char* L = (LAS char*)shm;
  const unsigned ldsbase = (unsigned)(size_t)L;
  const int tid = threadIdx.x, lane = tid & 63;
  const int wid = __builtin_amdgcn_readfirstlane(tid >> 6);
  const int lw = wid & 3;
  const int b = blockIdx.x, xcd = b & 7, r = b >> 3;
  const bool ffn = (r >> 4) == 0;
  const int ti = (xcd & 1) * 8 + (r & 7), cg = (xcd >> 1) * 2 + ((r >> 3) & 1);
  const int t64 = xcd * 4 + (r & 3), jc = r >> 2;
  const rsrc_t rws = __builtin_amdgcn_make_buffer_rsrc((void*)ws, (short)0, WS_END, 0x00020000);
  STAMP_DECL
  STAMP(0);
  const u64 tag0 = __hip_atomic_load((const GAS u64*)(ws + WS_EPOCH), RLX_AGENT);
  f32x16 acc[2][2];
  const int hh = lane >> 5;
  const int fB = 2 * ((lane >> 3) & 1) + hh;
  const int awB0 = (8 * hh + ((lane & 15) >> 2)) * 128 + ((((lane >> 4) & 1) ^ fB) * 32) + (lane & 3) * 8;
  const int awB1 = awB0 ^ 64;
  const int zb1 = (44 + 3 * lw) * 2048, zb2 = (56 + 6 * lw) * 2048, zb3 = (5 * lw) * 2048, zb4 = (20 + 4 * lw) * 2048;
  const unsigned hflag_off = WS_HFLAG + (((t64 >> 1) * 2 + (t64 & 1)) * 4 + lw) * 128 + (lane & 3) * 8;
  const unsigned hbase = WS_H + (t64 * 2 * 112 + 28 * lw) * 1024;

  if (wid >= 4) {
    const rsrc_t rx = __builtin_amdgcn_make_buffer_rsrc((void*)x, (short)0, 2048 * 512 * 2, 0x00020000);
    const rsrc_t rw = ffn ? __builtin_amdgcn_make_buffer_rsrc((void*)w1, (short)0, 512 * 1792 * 2, 0x00020000)
                          : __builtin_amdgcn_make_buffer_rsrc((void*)wqkv, (short)0, 512 * 1536 * 2, 0x00020000);
    const rsrc_t rw2 = __builtin_amdgcn_make_buffer_rsrc((void*)w2, (short)0, 1792 * 512 * 2, 0x00020000);
    const rsrc_t rwo = __builtin_amdgcn_make_buffer_rsrc((void*)wo, (short)0, 512 * 512 * 2, 0x00020000);
    const int RS = ffn ? 3584 : 3072, LSTR = ffn ? 128 : 1024;
    const int maincol = ffn ? (cg * 448 + (cg & 1) * 64) : cg * 128;
    const int sidecol = cg * 448 + ((cg & 1) ? 0 : 384);
    unsigned vx[2], vw[6], vs, vb[2];
    for (int p = 0; p < 2; p++) vx[p] = (lane >> 3) * 1024 + (((lane & 7) ^ (4 * p + (lane >> 4))) * 16);
    for (int e = 0; e < 6; e++) {
      const int n = 64 * e + lane, row = n / 24, cp = n % 24, f = 2 * ((row >> 1) & 1) + ((row >> 3) & 1);
      vw[e] = row * RS + (cp >> 3) * LSTR + (((cp & 7) ^ (f << 1)) * 16);
    }
    { const int row = lane >> 2; vs = row * 3584 + (((lane & 3) ^ (((row >> 3) & 1) << 1)) * 16); }
    for (int e = 0; e < 2; e++) vb[e] = (8 * e + (lane >> 3)) * 1024 + (((lane & 7) ^ ((2 * ((lane >> 4) & 1) + e) << 1)) * 16);
    auto issue = [&](int s, unsigned sl) {
      if (ffn) dma(sl + OFF_WS + lw * 1024, vs, rw, (s * 64 + 16 * lw) * 3584 + sidecol);
#pragma unroll
      for (int i = 0; i < 4; i++) dma(sl + (4 * lw + i) * 1024, vx[i & 1], rx, ti * 131072 + (4 * lw + i) * 8192 + s * 128);
#pragma unroll
      for (int e = 0; e < 6; e++) dma(sl + OFF_WM + (6 * lw + e) * 1024, vw[e], rw, (s * 64 + 16 * lw) * RS + maincol);
    };
    auto wblk_own = [&](int j, int zoff) {
      const unsigned so = (28 * lw + j) * 16384 + jc * 128;
      dma(ldsbase + zoff, vb[0], rw2, so); dma(ldsbase + zoff + 1024, vb[1], rw2, so);
    };
    auto wblk_c = [&](int j, int zoff) {
      if (j < 10) { const unsigned so = (28 * lw + 18 + j) * 16384 + jc * 128; dma(ldsbase + zoff, vb[0], rw2, so); dma(ldsbase + zoff + 1024, vb[1], rw2, so); }
      else { const unsigned so = (8 * lw + j - 10) * 16384 + jc * 128; dma(ldsbase + zoff, vb[0], rwo, so); dma(ldsbase + zoff + 1024, vb[1], rwo, so); }
    };
    s16x8 wr[18][2];
    auto wread = [&](int j, int zoff) { wr[j][0] = trfrag(L + zoff + awB0, 0, 512); wr[j][1] = trfrag(L + zoff + awB1, 0, 512); };

    issue(0, ldsbase + slot_base(0)); issue(1, ldsbase + slot_base(1));
    WAITV(10); BAR();
    STAMP(1);
    issue(2, ldsbase + slot_base(2));
#pragma unroll
    for (int s = 0; s < 5; s++) { WAITV(10); BAR(); issue(s + 3, ldsbase + slot_base(s % 3)); }
#pragma unroll
    for (int j = 0; j < 3; j++) wblk_own(j, zb1 + j * 2048);
    WAITV(16); BAR();
#pragma unroll
    for (int j = 0; j < 6; j++) wblk_own(3 + j, zb2 + j * 2048);
    WAITV(12);
#pragma unroll
    for (int j = 0; j < 3; j++) wread(j, zb1 + j * 2048);
    WAITL(); SCHED0();
#pragma unroll
    for (int j = 0; j < 3; j++) wblk_c(j, zb1 + j * 2048);
    BAR();
#pragma unroll
    for (int j = 0; j < 5; j++) wblk_own(9 + j, zb3 + j * 2048);
    WAITV(16);
#pragma unroll
    for (int j = 0; j < 6; j++) wread(3 + j, zb2 + j * 2048);
    WAITL(); SCHED0();
#pragma unroll
    for (int j = 0; j < 6; j++) wblk_c(3 + j, zb2 + j * 2048);
    BAR();
    STAMP(2);
#pragma unroll
    for (int j = 0; j < 4; j++) wblk_own(14 + j, zb4 + j * 2048);
    WAITV(20);
#pragma unroll
    for (int j = 0; j < 5; j++) wread(9 + j, zb3 + j * 2048);
    WAITL(); SCHED0();
#pragma unroll
    for (int j = 0; j < 5; j++) wblk_c(9 + j, zb3 + j * 2048);
    WAITV(10);
#pragma unroll
    for (int j = 0; j < 4; j++) wread(14 + j, zb4 + j * 2048);
    WAITL(); SCHED0();
#pragma unroll
    for (int j = 0; j < 4; j++) wblk_c(14 + j, zb4 + j * 2048);
    const u64 tag = (((u64)__builtin_amdgcn_readfirstlane((unsigned)(tag0 >> 32)) << 32) | __builtin_amdgcn_readfirstlane((unsigned)tag0)) + 1;
    WAITV(0);
    lds_signal(L, 8 + lw, tag);
    STAMP(3);
    if (!ffn) {
      const int hd = 4 * cg + lw;
      poll_flags((const GAS u64*)(ws + WS_KFLAG + hd * 128 + (lane & 15) * 8), tag);
      STAMP(4);
      v4u kp[16];
#pragma unroll
      for (int i = 0; i < 16; i++) kp[i] = ld_sc1(rws, lane * 16, WS_KVP + (hd * 16 + i) * 1024);
      const v4u zp = ld_sc1(rws, (lane & 15) * 128 + (lane >> 4) * 16, WS_ZP + hd * 2048);
      const int q = lane >> 4, rho = lane & 15;
      v4u sel[2];
#pragma unroll
      for (int u = 0; u < 2; u++)
#pragma unroll
        for (int jj = 0; jj < 4; jj++) {
          const int f0 = 16 * (q >> 1) + 8 * (jj >> 1) + 4 * (q & 1) + 2 * (jj & 1);
          sel[u][jj] = ((f0 == 16 * u + rho) ? 0x3f80u : 0u) | ((f0 + 1 == 16 * u + rho) ? 0x3f800000u : 0u);
        }
      f32x4 t0 = {0, 0, 0, 0}, t1 = {0, 0, 0, 0};
#pragma unroll
      for (int i = 0; i < 16; i++) { t0 = mfma16(sel[0], kp[i], t0); t1 = mfma16(sel[1], kp[i], t1); }
      LAS char* ks = L + SCR_KS + lw * 1088;
      *(LAS v2u*)(ks + rho * 64 + q * 8) = v2u{pk(t0[0], t0[1]), pk(t0[2], t0[3])};
      *(LAS v2u*)(ks + rho * 64 + 32 + q * 8) = v2u{pk(t1[0], t1[1]), pk(t1[2], t1[3])};
      float z[8];
#pragma unroll
      for (int jj = 0; jj < 4; jj++) { z[2 * jj] = __uint_as_float(zp[jj] << 16); z[2 * jj + 1] = __uint_as_float(zp[jj] & 0xffff0000u); }
#pragma unroll
      for (int jj = 0; jj < 8; jj++) {
        z[jj] += __uint_as_float(__builtin_amdgcn_update_dpp(0u, __float_as_uint(z[jj]), 0x128, 0xf, 0xf, false));
        z[jj] += __uint_as_float(__builtin_amdgcn_update_dpp(0u, __float_as_uint(z[jj]), 0x124, 0xf, 0xf, false));
        z[jj] += __uint_as_float(__builtin_amdgcn_update_dpp(0u, __float_as_uint(z[jj]), 0x122, 0xf, 0xf, false));
        z[jj] += __uint_as_float(__builtin_amdgcn_update_dpp(0u, __float_as_uint(z[jj]), 0x121, 0xf, 0xf, false));
      }
      if (rho == 0) {
        LAS char* zr = ks + 1024 + (16 * (q >> 1) + 4 * (q & 1)) * 2;
        *(LAS v2u*)(zr) = v2u{pk(z[0], z[1]), pk(z[2], z[3])};
        *(LAS v2u*)(zr + 16) = v2u{pk(z[4], z[5]), pk(z[6], z[7])};
      }
      lds_signal(L, 4 + lw, tag);
    }
    STAMP(5);
    poll_flags((const GAS u64*)(ws + hflag_off), tag);
    STAMP(6);
    v4u av[18][2];
    constexpr int W0 = 3;
#pragma unroll
    for (int j = 0; j < W0; j++) { av[j][0] = ld_sc1(rws, lane * 16, hbase + j * 1024); av[j][1] = ld_sc1(rws, lane * 16, hbase + (112 + j) * 1024); }
    SCHED0();
    acc[0][0] = zero16(); acc[0][1] = zero16(); acc[1][0] = zero16(); acc[1][1] = zero16();
#pragma unroll
    for (int j = 0; j < 18; j++) {
      acc[0][0] = mfma32(wr[j][0], av[j][0], acc[0][0]);
      acc[1][0] = mfma32(wr[j][1], av[j][0], acc[1][0]);
      acc[0][1] = mfma32(wr[j][0], av[j][1], acc[0][1]);
      acc[1][1] = mfma32(wr[j][1], av[j][1], acc[1][1]);
      SCHED0();
#pragma unroll
      for (int n = W0 + 2 * j; n < W0 + 2 * j + 2; n++)
        if (n < 18) { av[n][0] = ld_sc1(rws, lane * 16, hbase + n * 1024); av[n][1] = ld_sc1(rws, lane * 16, hbase + (112 + n) * 1024); }
      SCHED0();
    }
    STAMP(7);
    poll_flags((const GAS u64*)(ws + hflag_off + (WS_OFLAG - WS_HFLAG)), tag);
    {
      const unsigned go = WS_G + (t64 * 4 + lw) * 256;
      const float g0 = __uint_as_float(__builtin_amdgcn_raw_buffer_load_b32(rws, (lane & 31) * 4, go, 16));
      const float g1 = __uint_as_float(__builtin_amdgcn_raw_buffer_load_b32(rws, (lane & 31) * 4, go + 128, 16));
#pragma unroll
      for (int i = 0; i < 16; i++) { acc[0][0][i] *= g0; acc[1][0][i] *= g0; acc[0][1][i] *= g1; acc[1][1][i] *= g1; }
    }
    if (b == 0 && lw == 3) {
      const int w0 = lane, w1i = lane + 64;
      poll_flags((const GAS u64*)(ws + WS_HFLAG + ((w0 >> 3) * 8 + ((w0 & 7) >> 1)) * 128 + (w0 & 1) * 16), tag);
      poll_flags((const GAS u64*)(ws + WS_HFLAG + ((w1i >> 3) * 8 + ((w1i & 7) >> 1)) * 128 + (w1i & 1) * 16), tag);
      poll_flags((const GAS u64*)(ws + WS_KFLAG + (w0 >> 4) * 4 * 128 + (w0 & 15) * 8), tag);
      poll_flags((const GAS u64*)(ws + WS_KFLAG + (w1i >> 4) * 4 * 128 + (w1i & 15) * 8), tag);
      if (lane == 0) __hip_atomic_store((GAS u64*)(ws + WS_EPOCH), tag, RLX_AGENT);
    }
  } else {
    const int m = lw & 1, nh = lw >> 1, q = lane >> 4;
    u64 tag;
    if (ffn) {
      int ax0 = m * 8192 + (lane & 15) * 128 + ((q ^ ((lane & 15) >> 1)) * 16);
      int aw[6], as_;
      { const int f = 2 * ((lane >> 3) & 1) + (q & 1), rb = 8 * q + ((lane & 15) >> 2);
        for (int i = 0; i < 6; i++) { const int T = 6 * nh + i; aw[i] = OFF_WM + rb * 384 + (lane & 3) * 8 + (((T & 3) ^ f) * 32) + (T >> 2) * 128; }
        as_ = OFF_WS + rb * 64 + ((nh ^ (q & 1)) * 32) + (lane & 3) * 8; }
      f32x4 ca[7][4];
      for (int i = 0; i < 7; i++) for (int j = 0; j < 4; j++) ca[i][j] = f32x4{0, 0, 0, 0};
      s16x8 xf[2][4], wf[2][7];
      auto rd = [&](int sb, int u, int bi) {
        LAS char* S = L + sb;
#pragma unroll
        for (int t = 0; t < 4; t++) xf[bi][t] = *(LAS s16x8*)(S + (ax0 ^ (u * 64)) + t * 2048);
#pragma unroll
        for (int i = 0; i < 6; i++) wf[bi][i] = trfrag(S + aw[i], (32 * u) * 384, (32 * u + 4) * 384);
        wf[bi][6] = trfrag(S + as_, (32 * u) * 64, (32 * u + 4) * 64);
      };
      auto mm = [&](int bi) {
#pragma unroll
        for (int i = 0; i < 7; i++)
#pragma unroll
          for (int t = 0; t < 4; t++) ca[i][t] = mfma16(wf[bi][i], xf[bi][t], ca[i][t]);
#pragma unroll
        for (int i = 0; i < 18; i++) { __builtin_amdgcn_sched_group_barrier(0x8, 1, 0); __builtin_amdgcn_sched_group_barrier(0x100, 1, 0); }
        __builtin_amdgcn_sched_group_barrier(0x8, 10, 0);
      };
      tag = (((u64)__builtin_amdgcn_readfirstlane((unsigned)(tag0 >> 32)) << 32) | __builtin_amdgcn_readfirstlane((unsigned)tag0)) + 1;
      WAITV(0);
      BAR();
      STAMP(1);
      int sb = slot_base(0);
      rd(sb, 0, 0);
#pragma unroll 1
      for (int s = 0; s < 8; s++) {
        rd(sb, 1, 1); mm(0); SCHED0();
        BAR();
        sb = (sb == slot_base(0)) ? slot_base(1) : (sb == slot_base(1) ? slot_base(2) : slot_base(0));
        rd(sb, 0, 0); mm(1); SCHED0();
      }
      STAMP(2);
      const int kt_main = 14 * cg + ((cg & 1) ? 2 : 0) + 6 * nh, kt_side = 14 * cg + ((cg & 1) ? 0 : 12) + nh;
#pragma unroll
      for (int i = 0; i < 7; i++) {
        const int kt = (i < 6) ? kt_main + i : kt_side;
#pragma unroll
        for (int a = 0; a < 2; a++) {
          const f32x4 c0 = ca[i][2 * a], c1 = ca[i][2 * a + 1];
          const unsigned x0 = pk(fmaxf(c0[0], 0.f), fmaxf(c0[1], 0.f)), x1 = pk(fmaxf(c0[2], 0.f), fmaxf(c0[3], 0.f));
          const unsigned y0 = pk(fmaxf(c1[0], 0.f), fmaxf(c1[1], 0.f)), y1 = pk(fmaxf(c1[2], 0.f), fmaxf(c1[3], 0.f));
          const auto s0 = __builtin_amdgcn_permlane16_swap(x0, y0, false, false);
          const auto s1 = __builtin_amdgcn_permlane16_swap(x1, y1, false, false);
          st_sc1(v4u{s0[0], s1[0], s0[1], s1[1]}, rws, lane * 16, WS_H + ((ti * 4 + m * 2 + a) * 112 + kt) * 1024);
        }
      }
      STAMP(3);
      WAITV(0);
      if (lane == 0) __hip_atomic_store((GAS u64*)(ws + WS_HFLAG + ((ti * 2 + m) * 4 + (cg >> 1)) * 128 + ((cg & 1) * 2 + nh) * 8), tag, RLX_AGENT);
      STAMP(4);
    } else {
      const int p = nh;
      if (lw == 0) { *(LAS v4u*)(L + GATE_OFF + lane * 16) = *(const GAS v4u*)((const GAS char*)gw + lane * 16); }
      if (lw == 1) { *(LAS v4u*)(L + GATE_OFF + 1024 + lane * 16) = v4u{0, 0, 0, 0}; }
      v4u wp[2];
      { const int rho = lane & 31, f = (rho & 3) + 4 * (rho >> 3); const unsigned sg = ((rho >> 2) & 1) ? 0x80008000u : 0u;
#pragma unroll
        for (int s = 0; s < 2; s++) {
          const GAS u16* w = (const GAS u16*)wphi + (4 * cg + 2 * p + s) * 256 + (4 * hh) * 16 + f;
#pragma unroll
          for (int jj = 0; jj < 4; jj++) { const int d = 8 * (jj >> 1) + 2 * (jj & 1); wp[s][jj] = (((unsigned)w[d * 16]) | (((unsigned)w[(d + 1) * 16]) << 16)) ^ sg; }
        } }
      const int ax0 = m * 8192 + (lane & 31) * 128 + ((hh ^ (((lane & 31) >> 1) & 7)) * 16);
      const int awq = OFF_WM + (8 * hh + ((lane & 15) >> 2)) * 384 + (((2 * p + ((lane >> 4) & 1)) ^ fB) * 32) + (lane & 3) * 8;
      const int ag = GATE_OFF + (((q & 1) == ((lane >> 2) & 1)) ? 0 : 1024) + (q >> 1) * 16;
      f32x16 cq[2], ck[2], cv[2]; f32x4 cgate = {0, 0, 0, 0};
      for (int t = 0; t < 2; t++) { cq[t] = zero16(); ck[t] = zero16(); cv[t] = zero16(); }
      s16x8 xf[2][2][2], wf[2][2][3], gf[2][2];
      auto rd = [&](int sb, int goff, int u, int bi) {
        LAS char* S = L + sb;
#pragma unroll
        for (int k = 0; k < 2; k++) {
          const int s16 = 2 * u + k;
#pragma unroll
          for (int t = 0; t < 2; t++) xf[bi][k][t] = *(LAS s16x8*)(S + (ax0 ^ (s16 * 32)) + t * 4096);
#pragma unroll
          for (int i = 0; i < 3; i++) wf[bi][k][i] = trfrag(S + awq, (16 * s16) * 384 + i * 128, (16 * s16 + 4) * 384 + i * 128);
          gf[bi][k] = *(LAS s16x8*)(L + ag + goff + s16 * 32);
        }
      };
      auto mm = [&](int bi) {
#pragma unroll
        for (int k = 0; k < 2; k++) {
#pragma unroll
          for (int t = 0; t < 2; t++) {
            ck[t] = mfma32(wf[bi][k][1], xf[bi][k][t], ck[t]);
            cv[t] = mfma32(xf[bi][k][t], wf[bi][k][2], cv[t]);
            cq[t] = mfma32(wf[bi][k][0], xf[bi][k][t], cq[t]);
          }
          cgate = mfma16(gf[bi][k], p ? xf[bi][k][1] : xf[bi][k][0], cgate);
        }
#pragma unroll
        for (int i = 0; i < 9; i++) { __builtin_amdgcn_sched_group_barrier(0x8, 1, 0); __builtin_amdgcn_sched_group_barrier(0x100, 2, 0); }
        __builtin_amdgcn_sched_group_barrier(0x8, 5, 0);
      };
      tag = (((u64)__builtin_amdgcn_readfirstlane((unsigned)(tag0 >> 32)) << 32) | __builtin_amdgcn_readfirstlane((unsigned)tag0)) + 1;
      WAITV(0);
      BAR();
      STAMP(1);
      int sb = slot_base(0);
      rd(sb, 0, 0, 0);
#pragma unroll 1
      for (int s = 0; s < 8; s++) {
        rd(sb, s * 128, 1, 1); mm(0); SCHED0();
        BAR();
        sb = (sb == slot_base(0)) ? slot_base(1) : (sb == slot_base(1) ? slot_base(2) : slot_base(0));
        rd(sb, ((s + 1) & 7) * 128, 0, 0); mm(1); SCHED0();
      }
      STAMP(2);
      v4u idn[2];
#pragma unroll
      for (int s = 0; s < 2; s++)
#pragma unroll
        for (int jj = 0; jj < 4; jj++) { const int F = 16 * hh + 8 * s + 2 * jj, c = lane & 31; idn[s][jj] = ((c == F) ? 0x3f80u : 0u) | ((c == F + 1) ? 0x3f800000u : 0u); }
      f32x16 kv[2] = {zero16(), zero16()};
      const bool upper = (lane & 16) != 0;
#pragma unroll
      for (int t = 0; t < 2; t++) {
        const v4u v0 = pack8(cv[t], 0), v1 = pack8(cv[t], 1);
#pragma unroll
        for (int s = 0; s < 2; s++) {
          const f32x16 pkk = mfma32(wp[s], pack8(ck[t], s), zero16());
          v4u lo, hi; softmax16(pkk, lo, hi);
          f32x16 z = mfma32(lo, idn[0], zero16());
          z = mfma32(hi, idn[1], z);
          v4u e0, e1;
#pragma unroll
          for (int jj = 0; jj < 4; jj++) { e0[jj] = (upper == (s == 0)) ? 0x3f803f80u : v0[jj]; e1[jj] = (upper == (s == 0)) ? 0x3f803f80u : v1[jj]; }
          kv[s] = mfma32(pack8(z, 0), e0, kv[s]);
          kv[s] = mfma32(pack8(z, 1), e1, kv[s]);
        }
      }
      {
        const bool upo = (m == 0);
        const bool data_o = (upper == upo), zl_o = !data_o && (lane & 15) == 0;
        const f32x16 o = m ? kv[0] : kv[1];
        if (data_o || zl_o) {
          LAS f32x4* d = (LAS f32x4*)(L + SCR_XCH + lw * 2176 + (hh * 17 + (data_o ? (lane & 15) : 16)) * 64);
#pragma unroll
          for (int i = 0; i < 4; i++) d[i] = f32x4{o[4 * i], o[4 * i + 1], o[4 * i + 2], o[4 * i + 3]};
        }
        if (lane < 32) *(LAS float*)(L + SCR_GX + lw * 128 + lane * 4) = cgate[0];
        lds_signal(L, lw, tag);
        lds_wait(L, lw ^ 1, tag);
        const bool data_m = (upper == (m == 1)), zl_m = !data_m && (lane & 15) == 0;
        f32x16 own = m ? kv[1] : kv[0];
        const LAS f32x4* sp = (const LAS f32x4*)(L + SCR_XCH + (lw ^ 1) * 2176 + (hh * 17 + (data_m ? (lane & 15) : 16)) * 64);
#pragma unroll
        for (int i = 0; i < 4; i++) { const f32x4 v = sp[i]; own[4 * i] += v[0]; own[4 * i + 1] += v[1]; own[4 * i + 2] += v[2]; own[4 * i + 3] += v[3]; }
        const int hd = 4 * cg + 2 * p + m;
        const unsigned o0 = data_m ? (WS_KVP + (hd * 16 + ti) * 1024 + (hh * 16 + (lane & 15)) * 16) : (zl_m ? (WS_ZP + (hd * 16 + ti) * 128 + hh * 16) : OOB);
        const unsigned o1 = data_m ? o0 + 512 : (zl_m ? o0 + 32 : OOB);
        st_sc1(pack8(own, 0), rws, o0, 0);
        st_sc1(pack8(own, 1), rws, o1, 0);
        STAMP(3);
        WAITV(0);
        if (lane == 0) __hip_atomic_store((GAS u64*)(ws + WS_KFLAG + hd * 128 + ti * 8), tag, RLX_AGENT);
      }
      STAMP(4);
      v4u pq[2][2][2];
#pragma unroll
      for (int t = 0; t < 2; t++)
#pragma unroll
        for (int s = 0; s < 2; s++) { const f32x16 pp = mfma32(wp[s], pack8(cq[t], s), zero16()); softmax16(pp, pq[t][s][0], pq[t][s][1]);
          asm volatile("" : "+v"(pq[t][s][0]), "+v"(pq[t][s][1])); }
      lds_wait(L, lw ^ 2, tag);
      float gt[2];
      { const float other = *(const LAS float*)(L + SCR_GX + (lw ^ 2) * 128 + (lane & 31) * 4);
        const float l0 = p ? other : cgate[0], l1 = p ? cgate[0] : other;
        gt[0] = __builtin_amdgcn_rcpf(1.f + __builtin_amdgcn_exp2f(-1.4426950408889634f * l0));
        gt[1] = __builtin_amdgcn_rcpf(1.f + __builtin_amdgcn_exp2f(-1.4426950408889634f * l1)); }
      STAMP(5);
      { const int rho = lane & 31, krow = rho < 16 ? (8 * ((rho >> 2) & 1) + (rho & 3) + 4 * (rho >> 3)) : 16;
#pragma unroll
        for (int s = 0; s < 2; s++) {
          lds_wait(L, 4 + 2 * p + s, tag);
          const LAS char* ks = L + SCR_KS + (2 * p + s) * 1088 + krow * 64 + hh * 32;
          const v4u k0 = *(const LAS v4u*)ks, k1 = *(const LAS v4u*)(ks + 16);
#pragma unroll
          for (int t = 0; t < 2; t++) {
            f32x16 o = mfma32(k0, pq[t][s][0], zero16());
            o = mfma32(k1, pq[t][s][1], o);
            const float sc = gt[t] * __builtin_amdgcn_rcpf(o[8] + 1e-6f);
#pragma unroll
            for (int i = 0; i < 8; i++) o[i] *= sc;
            st_sc1(pack8(o, 0), rws, lane * 16, WS_O + ((ti * 4 + m * 2 + t) * 32 + 4 * cg + 2 * p + s) * 1024);
          }
        }
        const unsigned go = ((cg & 1) == 0 && p == 0) ? (WS_G + ((ti * 2 + m) * 4 + (cg >> 1)) * 256 + lane * 4) : OOB;
        __builtin_amdgcn_raw_buffer_store_b32(__float_as_uint(1.f - (hh ? gt[1] : gt[0])), rws, go, 0, 16);
        WAITV(0);
        if (lane == 0) __hip_atomic_store((GAS u64*)(ws + WS_OFLAG + ((ti * 2 + m) * 4 + (cg >> 1)) * 128 + ((cg & 1) * 2 + p) * 8), tag, RLX_AGENT);
      }
    }
    STAMP(6);
    poll_flags((const GAS u64*)(ws + hflag_off), tag);
    STAMP(7);
    v4u av[10][2];
#pragma unroll
    for (int j = 0; j < 10; j++) { av[j][0] = ld_sc1(rws, lane * 16, hbase + (18 + j) * 1024); av[j][1] = ld_sc1(rws, lane * 16, hbase + (112 + 18 + j) * 1024); }
    SCHED0();
    lds_wait(L, 8 + lw, tag);
    STAMP(8);
    acc[0][0] = zero16(); acc[0][1] = zero16(); acc[1][0] = zero16(); acc[1][1] = zero16();
    auto zoff = [&](int j) { return j < 3 ? zb1 + j * 2048 : (j < 9 ? zb2 + (j - 3) * 2048 : (j < 14 ? zb3 + (j - 9) * 2048 : zb4 + (j - 14) * 2048)); };
#pragma unroll
    for (int j = 0; j < 10; j++) {
      const s16x8 w0 = trfrag(L + zoff(j) + awB0, 0, 512), w1f = trfrag(L + zoff(j) + awB1, 0, 512);
      acc[0][0] = mfma32(w0, av[j][0], acc[0][0]);
      acc[1][0] = mfma32(w1f, av[j][0], acc[1][0]);
      acc[0][1] = mfma32(w0, av[j][1], acc[0][1]);
      acc[1][1] = mfma32(w1f, av[j][1], acc[1][1]);
    }
    SCHED0();
    STAMP(9);
    poll_flags((const GAS u64*)(ws + hflag_off + (WS_OFLAG - WS_HFLAG)), tag);
    STAMP(10);
    v4u ov[8][2];
    const unsigned go = WS_G + (t64 * 4 + lw) * 256;
    const float g0 = __uint_as_float(__builtin_amdgcn_raw_buffer_load_b32(rws, (lane & 31) * 4, go, 16));
    const float g1 = __uint_as_float(__builtin_amdgcn_raw_buffer_load_b32(rws, (lane & 31) * 4, go + 128, 16));
    const unsigned obase = WS_O + (t64 * 2 * 32 + 8 * lw) * 1024;
#pragma unroll
    for (int j = 0; j < 8; j++) { ov[j][0] = ld_sc1(rws, lane * 16, obase + j * 1024); ov[j][1] = ld_sc1(rws, lane * 16, obase + (32 + j) * 1024); }
    SCHED0();
#pragma unroll
    for (int i = 0; i < 16; i++) { acc[0][0][i] *= g0; acc[1][0][i] *= g0; acc[0][1][i] *= g1; acc[1][1][i] *= g1; }
#pragma unroll
    for (int j = 0; j < 8; j++) {
      const s16x8 w0 = trfrag(L + zoff(10 + j) + awB0, 0, 512), w1f = trfrag(L + zoff(10 + j) + awB1, 0, 512);
      acc[0][0] = mfma32(w0, ov[j][0], acc[0][0]);
      acc[1][0] = mfma32(w1f, ov[j][0], acc[1][0]);
      acc[0][1] = mfma32(w0, ov[j][1], acc[0][1]);
      acc[1][1] = mfma32(w1f, ov[j][1], acc[1][1]);
    }
  }
  STAMP(11);
  BAR();
  {
    LAS char* wbase = L + wid * 2048 + lane * 16;
#pragma unroll
    for (int d = 0; d < 8; d++) {
      const f32x16& t = acc[d >> 2][(d >> 1) & 1];
      const int o = 8 * (d & 1);
      if (d != wid) {
        *(LAS f32x4*)(wbase + d * 16384) = f32x4{t[o], t[o + 1], t[o + 2], t[o + 3]};
        *(LAS f32x4*)(wbase + d * 16384 + 1024) = f32x4{t[o + 4], t[o + 5], t[o + 6], t[o + 7]};
      }
    }
    f32x4 s0, s1;
    {
      const f32x16 t = (wid & 4) ? ((wid & 2) ? acc[1][1] : acc[1][0]) : ((wid & 2) ? acc[0][1] : acc[0][0]);
      s0 = (wid & 1) ? f32x4{t[8], t[9], t[10], t[11]} : f32x4{t[0], t[1], t[2], t[3]};
      s1 = (wid & 1) ? f32x4{t[12], t[13], t[14], t[15]} : f32x4{t[4], t[5], t[6], t[7]};
    }
    BAR();
    STAMP(12);
    const LAS char* rbase = L + wid * 16384 + lane * 16;
#pragma unroll
    for (int sidx = 1; sidx < 8; sidx++) {
      const int src = (wid + sidx) & 7;
      s0 += *(const LAS f32x4*)(rbase + src * 2048);
      s1 += *(const LAS f32x4*)(rbase + src * 2048 + 1024);
    }
    const unsigned a0 = pk(s0[0], s0[1]), a1 = pk(s0[2], s0[3]), b0 = pk(s1[0], s1[1]), b1 = pk(s1[2], s1[3]);
    const auto r0 = __builtin_amdgcn_permlane32_swap(a0, b0, false, false);
    const auto r1 = __builtin_amdgcn_permlane32_swap(a1, b1, false, false);
    const int tok = t64 * 64 + ((wid >> 1) & 1) * 32 + (lane & 31);
    const int col = jc * 64 + (wid >> 2) * 32 + (wid & 1) * 16 + hh * 8;
    *(GAS v4u*)((GAS u16*)out + tok * 512 + col) = v4u{r0[0], r1[0], r0[1], r1[1]};
  }
  STAMP(13);
  STAMP_FLUSH;
}

#include <cstdio>
#define ROLE_OF(b) (((b)>>7)&1)
__global__ void diag_print(const unsigned* d) {
  __shared__ int res[64][3];
  unsigned t0 = d[0];
  for (int i = 0; i < 2048; i++) if ((int)(d[i * 16] - t0) < 0) t0 = d[i * 16];
  const int role = threadIdx.x >> 4, st = threadIdx.x & 15;
  int mn = 1 << 30, mx = -1, med = -1, n = 0;
  if (st < 14) {
    for (int b = 0; b < 256; b++) { if (ROLE_OF(b) != (role >> 1)) continue; for (int w = 0; w < 4; w++) { unsigned t = d[(b * 8 + w + (role & 1) * 4) * 16 + st]; if (!t) continue; int v = (int)(t - t0); mn = min(mn, v); mx = max(mx, v); n++; } }
    if (n == 512) { int lo = mn, hi = mx; while (lo < hi) { int mid = (lo + hi) / 2, c = 0;
        for (int b = 0; b < 256; b++) { if (ROLE_OF(b) != (role >> 1)) continue; for (int w = 0; w < 4; w++) { int v = (int)(d[(b * 8 + w + (role & 1) * 4) * 16 + st] - t0); c += v <= mid; } }
        if (c >= 256) hi = mid; else lo = mid + 1; } med = lo; }
  }
  res[threadIdx.x][0] = mn; res[threadIdx.x][1] = med; res[threadIdx.x][2] = mx;
  __syncthreads();
  if (threadIdx.x == 0) {
    for (int r = 0; r < 4; r++) { printf("role%d:", r); for (int s = 0; s < 14; s++) if (res[r * 16 + s][1] >= 0) printf(" %d:[%d %d %d]", s, res[r * 16 + s][0], res[r * 16 + s][1], res[r * 16 + s][2]); printf("\n"); }
    printf("clock %f GHz\n", (double)(d[15] - d[14]) / (double)(d[13] - d[0]) * 0.1);
  }
}
#define DIAG_LAUNCH diag_print<<<1, 64, 0, stream>>>((const unsigned*)((char*)d_ws + WS_DIAG));

extern "C" void kernel_launch(void* const* d_in, const int* in_sizes, int n_in, void* d_out, int out_size, void* d_ws, size_t ws_size,
                              hipStream_t stream) {
  (void)hipFuncSetAttribute((const void*)fused_kernel, hipFuncAttributeMaxDynamicSharedMemorySize, LDS_BYTES);
  fused_kernel<<<256, 512, LDS_BYTES, stream>>>((const u16*)d_in[0], (const u16*)d_in[1], (const u16*)d_in[2], (const u16*)d_in[3],
                                                (const u16*)d_in[5], (const u16*)d_in[6], (const u16*)d_in[7], (u16*)d_out, (char*)d_ws);
  DIAG_LAUNCH
}
```

```cpp
#include <hip/hip_runtime.h>
#include <hip/hip_bf16.h>

typedef unsigned short u16;
typedef unsigned long long u64;
typedef unsigned v4u __attribute__((ext_vector_type(4)));
typedef unsigned v2u __attribute__((ext_vector_type(2)));
typedef short s16x4 __attribute__((ext_vector_type(4)));
typedef short s16x8 __attribute__((ext_vector_type(8)));
typedef __bf16 bf16x8 __attribute__((ext_vector_type(8)));
typedef __bf16 bf16x2 __attribute__((ext_vector_type(2)));
typedef float f32x2 __attribute__((ext_vector_type(2)));
typedef float f32x4 __attribute__((ext_vector_type(4)));
typedef float f32x16 __attribute__((ext_vector_type(16)));
typedef __amdgpu_buffer_rsrc_t rsrc_t;
#define LAS __attribute__((address_space(3)))
#define GAS __attribute__((address_space(1)))
#define DI __device__ __forceinline__
#define RLX_AGENT __ATOMIC_RELAXED, __HIP_MEMORY_SCOPE_AGENT

#define DIAG 0
#define PFW 0
#define HOLD 1
#define ROTA 0
#define ROTB 1
#define MAPB 0
#define OUTSC 0

constexpr unsigned WS_EPOCH = 0;
constexpr unsigned WS_HFLAG = 4096;
constexpr unsigned WS_OFLAG = WS_HFLAG + 128 * 128;
constexpr unsigned WS_KFLAG = WS_OFLAG + 128 * 128;
constexpr unsigned WS_G     = WS_KFLAG + 32 * 128;
constexpr unsigned WS_ZP    = WS_G + 128 * 256;
constexpr unsigned WS_KVP   = WS_ZP + 512 * 128;
constexpr unsigned WS_O     = WS_KVP + 512 * 1024;
constexpr unsigned WS_H     = WS_O + 64 * 32 * 1024;
constexpr unsigned WS_END   = WS_H + 64 * 112 * 1024;
constexpr unsigned WS_DIAG  = WS_END;
constexpr unsigned OOB = 0x7ffffff0u;

constexpr int SLOT = 45056, OFF_WM = 16384, OFF_WS = 40960;
constexpr int LDS_BYTES = 163840;
__host__ __device__ constexpr int slot_base(int i) { return i == 0 ? 0 : (i == 1 ? SLOT : 58 * 2048); }
constexpr int GATE_OFF = OFF_WS;
constexpr int WPHI_OFF = OFF_WS + 1024;
constexpr int PF_OFF = 56 * 2048;
constexpr int SCR = 36 * 2048;
constexpr int SCR_XCH = SCR;
constexpr int SCR_KS = SCR + 8704;
constexpr int SCR_GX = SCR_KS + 4352;
constexpr int SCR_FLAG = SCR_GX + 512;
constexpr int SCR_G2 = SCR_FLAG + 256;
constexpr unsigned SPIN_CAP = 1u << 16;

DI void dma(unsigned lds, int imm, unsigned voff, rsrc_t r, unsigned soff) {
  asm volatile("s_add_i32 m0, %0, %4\n\ts_nop 0\n\tbuffer_load_dwordx4 %1, %2, %3 offen lds" ::"s"(lds), "v"(voff), "s"(r), "s"(soff), "i"(imm) : "memory", "scc");
}
#define OPAQUE_S(x) asm volatile("" : "+s"(x))
DI s16x8 trfrag(LAS char* p, int off0, int off1) {
  s16x4 a = __builtin_amdgcn_ds_read_tr16_b64_v4i16((LAS s16x4*)(p + off0));
  s16x4 b = __builtin_amdgcn_ds_read_tr16_b64_v4i16((LAS s16x4*)(p + off1));
  return __builtin_shufflevector(a, b, 0, 1, 2, 3, 4, 5, 6, 7);
}
template <class A, class B> DI f32x4 mfma16(A a, B b, f32x4 c) {
  return __builtin_amdgcn_mfma_f32_16x16x32_bf16(__builtin_bit_cast(bf16x8, a), __builtin_bit_cast(bf16x8, b), c, 0, 0, 0);
}
template <class A, class B> DI f32x16 mfma32(A a, B b, f32x16 c) {
  return __builtin_amdgcn_mfma_f32_32x32x16_bf16(__builtin_bit_cast(bf16x8, a), __builtin_bit_cast(bf16x8, b), c, 0, 0, 0);
}
DI unsigned pk(float a, float b) { f32x2 v = {a, b}; return __builtin_bit_cast(unsigned, __builtin_convertvector(v, bf16x2)); }
typedef short s16x2 __attribute__((ext_vector_type(2)));
DI unsigned relu2(unsigned v) { return __builtin_bit_cast(unsigned, __builtin_elementwise_max(__builtin_bit_cast(s16x2, v), s16x2{0, 0})); }
DI v4u pack44(const f32x4& a, const f32x4& b) { return v4u{pk(a[0], a[1]), pk(a[2], a[3]), pk(b[0], b[1]), pk(b[2], b[3])}; }
DI v4u to_frag32(const f32x4& c0, const f32x4& c1) {
  const auto s0 = __builtin_amdgcn_permlane16_swap(pk(c0[0], c0[1]), pk(c1[0], c1[1]), false, false);
  const auto s1 = __builtin_amdgcn_permlane16_swap(pk(c0[2], c0[3]), pk(c1[2], c1[3]), false, false);
  return v4u{s0[0], s1[0], s0[1], s1[1]};
}
DI v4u pack8(const f32x16& x, int s) {
  return v4u{pk(x[8 * s], x[8 * s + 1]), pk(x[8 * s + 2], x[8 * s + 3]), pk(x[8 * s + 4], x[8 * s + 5]), pk(x[8 * s + 6], x[8 * s + 7])};
}
DI f32x16 zero16() { f32x16 z; for (int i = 0; i < 16; i++) z[i] = 0.f; return z; }
#define WAITV(n) asm volatile("s_waitcnt vmcnt(" #n ")" ::: "memory")
#define WAITL() asm volatile("s_waitcnt lgkmcnt(0)" ::: "memory")
#define BAR() do { asm volatile("s_waitcnt lgkmcnt(0)" ::: "memory"); __builtin_amdgcn_s_barrier(); asm volatile("" ::: "memory"); } while (0)
#define SCHED0() __builtin_amdgcn_sched_barrier(0)
DI v4u ld_sc1(rsrc_t r, unsigned voff, unsigned soff) { return __builtin_amdgcn_raw_buffer_load_b128(r, voff, soff, 16); }
DI void st_sc1(v4u v, rsrc_t r, unsigned voff, unsigned soff) { __builtin_amdgcn_raw_buffer_store_b128(v, r, voff, soff, 16); }

DI void poll_flags(const GAS u64* p, u64 tag) {
#pragma unroll 1
  for (unsigned spins = 0; spins < SPIN_CAP; ++spins) {
    u64 v = __hip_atomic_load(p, RLX_AGENT);
    if (__all(v == tag)) break;
    __builtin_amdgcn_s_sleep(1);
  }
  __builtin_amdgcn_fence(__ATOMIC_ACQUIRE, "wavefront");
  asm volatile("" ::: "memory");
}
DI void publish_then_poll(GAS u64* own, const GAS u64* p, u64 tag, int lane) {
  u64 v = __hip_atomic_load(p, RLX_AGENT);
  asm volatile("s_waitcnt vmcnt(0)" : "+v"(v) :: "memory");
  if (lane == 0) __hip_atomic_store(own, tag, RLX_AGENT);
  if (!__all(v == tag)) poll_flags(p, tag);
  __builtin_amdgcn_fence(__ATOMIC_ACQUIRE, "wavefront");
  asm volatile("" ::: "memory");
}
DI void touch(unsigned lds, unsigned voff, rsrc_t r) {
  asm volatile("s_mov_b32 m0, %0\n\ts_nop 0\n\tbuffer_load_dword %1, %2, 0 offen lds" ::"s"(lds), "v"(voff), "s"(r) : "memory");
}
DI u64 load_epoch(const char* ws) {
  u64 e;
  asm volatile("s_load_dwordx2 %0, %1, 0x0\n\ts_waitcnt lgkmcnt(0)" : "=s"(e) : "s"(ws) : "memory");
  return e + 1;
}
DI float row_sum16(float x) {
  x += __uint_as_float(__builtin_amdgcn_mov_dpp(__float_as_uint(x), 0x128, 0xf, 0xf, true));
  x += __uint_as_float(__builtin_amdgcn_mov_dpp(__float_as_uint(x), 0x124, 0xf, 0xf, true));
  x += __uint_as_float(__builtin_amdgcn_mov_dpp(__float_as_uint(x), 0x122, 0xf, 0xf, true));
  x += __uint_as_float(__builtin_amdgcn_mov_dpp(__float_as_uint(x), 0x121, 0xf, 0xf, true));
  return x;
}
DI void lds_wait(LAS char* L, int idx, u64 tag) {
  volatile LAS u64* f = (volatile LAS u64*)(L + SCR_FLAG + idx * 8);
#pragma unroll 1
  for (unsigned spins = 0; spins < (SPIN_CAP << 4); ++spins) {
    u64 v = *f;
    if (__all(v == tag)) break;
    __builtin_amdgcn_s_sleep(1);
  }
  asm volatile("" ::: "memory");
}
DI void lds_signal(LAS char* L, int idx, u64 tag) {
  asm volatile("s_waitcnt lgkmcnt(0)" ::: "memory");
  *(volatile LAS u64*)(L + SCR_FLAG + idx * 8) = tag;
}
#define STAMP(i) do {} while (0)
#define STAMPC(i) do {} while (0)
#define STAMP_DECL
#define STAMP_FLUSH

DI void softmax16x4(f32x16 (&p)[4], v4u (&o)[4][2]) {
  const float L2E = 1.4426950408889634f;
  float m[4], s0[4], s1[4];
#pragma unroll
  for (int u = 0; u < 4; u++) m[u] = fmaxf(fmaxf(p[u][0], p[u][1]), p[u][2]);
#pragma unroll
  for (int i = 3; i < 15; i += 2)
#pragma unroll
    for (int u = 0; u < 4; u++) m[u] = fmaxf(fmaxf(m[u], p[u][i]), p[u][i + 1]);
#pragma unroll
  for (int u = 0; u < 4; u++) m[u] = -L2E * fmaxf(m[u], p[u][15]);
#pragma unroll
  for (int i = 0; i < 16; i++)
#pragma unroll
    for (int u = 0; u < 4; u++) p[u][i] = __builtin_amdgcn_exp2f(__builtin_fmaf(p[u][i], L2E, m[u]));
#pragma unroll
  for (int u = 0; u < 4; u++) { s0[u] = p[u][0]; s1[u] = p[u][1]; }
#pragma unroll
  for (int i = 2; i < 16; i += 2)
#pragma unroll
    for (int u = 0; u < 4; u++) { s0[u] += p[u][i]; s1[u] += p[u][i + 1]; }
#pragma unroll
  for (int u = 0; u < 4; u++) s0[u] = __builtin_amdgcn_rcpf(s0[u] + s1[u]);
#pragma unroll
  for (int u = 0; u < 4; u++) {
#pragma unroll
    for (int i = 0; i < 16; i++) p[u][i] *= s0[u];
    o[u][0] = pack8(p[u], 0); o[u][1] = pack8(p[u], 1);
  }
}

__global__ __launch_bounds__(512, 2) void fused_kernel(const u16* __restrict__ x, const u16* __restrict__ wqkv, const u16* __restrict__ wphi,
                                                       const u16* __restrict__ wo, const u16* __restrict__ w1, const u16* __restrict__ w2,
                                                       const u16* __restrict__ gw, u16* __restrict__ out, char* __restrict__ ws) {
  extern __shared__ __attribute__((aligned(16))) char shm[];
  LAS char* L = (LAS char*)shm;
  const unsigned ldsbase = (unsigned)(size_t)L;
  const int tid = threadIdx.x, lane = tid & 63;
  const int wid = __builtin_amdgcn_readfirstlane(tid >> 6);
  const int lw = wid & 3;
  const int b = blockIdx.x, xcd = b & 7, r = b >> 3;
  const bool ffn = (r >> 4) == 0;
  const int ti = (xcd & 1) * 8 + (r & 7), cg = (xcd >> 1) * 2 + ((r >> 3) & 1);
  const int t64 = MAPB ? (xcd >> 1) * 8 + (r & 7) : xcd * 4 + (r & 3);
  const int jc = MAPB ? (xcd & 1) * 4 + (r >> 3) : r >> 2;
  const int rota = ROTA ? ((((r & 7) + 2 * (r >> 3)) & (ROTA - 1)) * (8 / (ROTA ? ROTA : 1))) : 0;
  int rotb = ROTB ? (MAPB ? (7 * (r >> 3) + ((7 * (r & 7)) >> 1)) % 28 : (7 * (r & 3) + ((7 * (r >> 2)) >> 1)) % 28) : 0;
  const int rotc = ROTB ? (MAPB ? (2 * (r >> 3) + (r & 7)) & 7 : (2 * (r & 3) + (r >> 2)) & 7) : 0;
  const rsrc_t rws = __builtin_amdgcn_make_buffer_rsrc((void*)ws, (short)0, WS_END, 0x00020000);
#define RX __builtin_amdgcn_make_buffer_rsrc((void*)x, (short)0, 2048 * 512 * 2, 0x00020000)
#define RW1 __builtin_amdgcn_make_buffer_rsrc((void*)w1, (short)0, 512 * 1792 * 2, 0x00020000)
#define RWQ __builtin_amdgcn_make_buffer_rsrc((void*)wqkv, (short)0, 512 * 1536 * 2, 0x00020000)
#define RW2 __builtin_amdgcn_make_buffer_rsrc((void*)w2, (short)0, 1792 * 512 * 2, 0x00020000)
#define RWO __builtin_amdgcn_make_buffer_rsrc((void*)wo, (short)0, 512 * 512 * 2, 0x00020000)
  STAMP_DECL
  STAMP(0);
  u64 tag;
  f32x16 acc[2][2];
  const int hh = lane >> 5;
  const int fB = 2 * ((lane >> 3) & 1) + hh;
  const int awB0 = (8 * hh + ((lane & 15) >> 2)) * 128 + ((((lane >> 4) & 1) ^ fB) * 32) + (lane & 3) * 8;
  const int awB1 = awB0 ^ 64;
  const int zb1 = (44 + 3 * lw) * 2048, zb2 = (56 + 6 * lw) * 2048, zb3 = (5 * lw) * 2048, zb4 = (20 + 4 * lw) * 2048;
  const unsigned hflag_off = WS_HFLAG + (t64 * 4 + lw) * 128 + (lane & 3) * 8;
  const unsigned hbase = WS_H + (t64 * 2 * 112 + 28 * lw) * 1024;
  auto ktf = [&](int j) { if (ROTB) OPAQUE_S(rotb); const int k = j + rotb; return k >= 28 ? k - 28 : k; };

  if (wid >= 4) {
    const rsrc_t rx = RX, rw2 = RW2, rwo = RWO;
    const rsrc_t rw = __builtin_amdgcn_make_buffer_rsrc((void*)(ffn ? w1 : wqkv), (short)0, ffn ? 512 * 1792 * 2 : 512 * 1536 * 2, 0x00020000);
    const int RS = ffn ? 3584 : 3072, LSTR = ffn ? 128 : 1024;
    const int maincol = ffn ? (cg * 448 + (cg & 1) * 64) : cg * 128;
    const int sidecol = cg * 448 + ((cg & 1) ? 0 : 384);
    unsigned vx[2], vw[6], vs, vb[2];
    for (int p = 0; p < 2; p++) vx[p] = (lane >> 3) * 1024 + (((lane & 7) ^ (4 * p + (lane >> 4))) * 16);
    { const int row = lane >> 2; vs = row * 3584 + (((lane & 3) ^ (((row >> 3) & 1) << 1)) * 16); }
    const unsigned lb1 = ldsbase + lw * 1024, lb4 = ldsbase + lw * 4096, lb6 = ldsbase + lw * 6144;
    unsigned sx = ti * 131072 + lw * 32768, sw = 16 * lw * RS + maincol, ss = 16 * lw * 3584 + sidecol;
    auto issue_x = [&](int s, int sl) {
      const int sr = (s + rota) & 7;
      OPAQUE_S(sx); OPAQUE_S(ss);
      if (ffn) dma(lb1, sl + OFF_WS, vs, rw, ss + sr * 64 * 3584);
#pragma unroll
      for (int i = 0; i < 4; i++) dma(lb4, sl + i * 1024, vx[i & 1], rx, sx + i * 8192 + sr * 128);
    };
    auto issue_w = [&](int s, int sl) {
      const int sr = (s + rota) & 7;
      OPAQUE_S(sw);
      const unsigned so = sw + sr * 64 * RS;
#pragma unroll
      for (int e = 0; e < 6; e++) dma(lb6, sl + OFF_WM + e * 1024, vw[e], rw, so);
    };
    auto issue = [&](int s, int sl) { issue_x(s, sl); issue_w(s, sl); };
    if (!ffn && lw < 3) {
      const rsrc_t rt = __builtin_amdgcn_make_buffer_rsrc((void*)(lw == 0 ? gw : wphi), (short)0, lw == 0 ? 1024 : 32 * 256 * 2, 0x00020000);
      dma(lb1, GATE_OFF, lane * 16, rt, lw ? cg * 2048 + (lw - 1) * 1024 : 0);
    }
    SCHED0();
    issue_x(0, slot_base(0));
    SCHED0();
    for (int e = 0; e < 6; e++) {
      const int n = 64 * e + lane, row = n / 24, cp = n % 24, f = 2 * ((row >> 1) & 1) + ((row >> 3) & 1);
      vw[e] = row * RS + (cp >> 3) * LSTR + (((cp & 7) ^ (f << 1)) * 16);
    }
    issue_w(0, slot_base(0));
    issue(1, slot_base(1));
    for (int e = 0; e < 2; e++) vb[e] = (8 * e + (lane >> 3)) * 1024 + (((lane & 7) ^ ((2 * ((lane >> 4) & 1) + e) << 1)) * 16);
    const unsigned s2 = 28 * lw * 16384 + jc * 128, so8 = 8 * lw * 16384 + jc * 128;
    auto wblk_own = [&](int j, int zb, int zo) {
      const unsigned so = s2 + ktf(j) * 16384;
      dma(ldsbase + zb, zo, vb[0], rw2, so); dma(ldsbase + zb, zo + 1024, vb[1], rw2, so);
    };
    auto wblk_c = [&](int j, int zb, int zo) {
      if (j < 10) { const unsigned so = s2 + ktf(18 + j) * 16384; dma(ldsbase + zb, zo, vb[0], rw2, so); dma(ldsbase + zb, zo + 1024, vb[1], rw2, so); }
      else { const unsigned so = so8 + ((j - 10 + rotc) & 7) * 16384; dma(ldsbase + zb, zo, vb[0], rwo, so); dma(ldsbase + zb, zo + 1024, vb[1], rwo, so); }
    };
    s16x8 wr[18][2];
    auto wread = [&](int j, int zoff) { wr[j][0] = trfrag(L + zoff + awB0, 0, 512); wr[j][1] = trfrag(L + zoff + awB1, 0, 512); };

    tag = load_epoch(ws);
    WAITV(10); BAR();
    STAMP(1);
    const unsigned pl = ldsbase + PF_OFF + lw * 256;
    auto tw = [&](int i) {
      if (!PFW) return;
      if (i < 4) {
        const int nl = MAPB ? 14 : 28, it = ((i * 32 + r) * 4 + lw) * nl + lane;
        touch(pl, lane < nl ? (MAPB ? (it >> 2) * 1024 + ((xcd & 1) * 4 + (it & 3)) * 128 : it * 128) : OOB, rw2);
      } else {
        const int nl = MAPB ? 16 : 32, it = (r * 4 + lw) * nl + lane;
        touch(pl, lane < nl ? (MAPB ? (it >> 2) * 1024 + ((xcd & 1) * 4 + (it & 3)) * 128 : it * 128) : OOB, rwo);
      }
    };
    issue(2, slot_base(2)); tw(0);
#pragma unroll
    for (int s = 0; s < 5; s++) {
      if (!PFW) WAITV(10); else if (s == 0) WAITV(11); else WAITV(12);
      BAR(); issue(s + 3, slot_base(s % 3));
      if (s < 4) tw(s + 1);
    }
#pragma unroll
    for (int j = 0; j < 3; j++) wblk_own(j, zb1, j * 2048);
    WAITV(16); BAR();
#pragma unroll
    for (int j = 0; j < 6; j++) wblk_own(3 + j, zb2, j * 2048);
    WAITV(18); BAR();
#pragma unroll
    for (int j = 0; j < 5; j++) wblk_own(9 + j, zb3, j * 2048);
    BAR();
    STAMP(2); STAMPC(0);
#pragma unroll
    for (int j = 0; j < 4; j++) wblk_own(14 + j, zb4, j * 2048);
    WAITV(18);
    STAMPC(1);
#pragma unroll
    for (int j = 0; j < 3; j++) wread(j, zb1 + j * 2048);
#pragma unroll
    for (int j = 0; j < 6; j++) wread(3 + j, zb2 + j * 2048);
    WAITL(); SCHED0();
    if (HOLD && ffn) lds_wait(L, 16 + lw, tag);
    STAMPC(2);
#pragma unroll
    for (int j = 0; j < 3; j++) wblk_c(j, zb1, j * 2048);
#pragma unroll
    for (int j = 0; j < 6; j++) wblk_c(3 + j, zb2, j * 2048);
    STAMPC(3);
    WAITV(18);
    STAMPC(4);
#pragma unroll
    for (int j = 0; j < 5; j++) wread(9 + j, zb3 + j * 2048);
#pragma unroll
    for (int j = 0; j < 4; j++) wread(14 + j, zb4 + j * 2048);
    WAITL(); SCHED0();
    STAMPC(5);
#pragma unroll
    for (int j = 0; j < 5; j++) wblk_c(9 + j, zb3, j * 2048);
#pragma unroll
    for (int j = 0; j < 4; j++) wblk_c(14 + j, zb4, j * 2048);
    STAMP(3); STAMPC(6);
    {
      u64 v = __hip_atomic_load((const GAS u64*)(ws + hflag_off), RLX_AGENT);
      asm volatile("s_waitcnt vmcnt(0)" : "+v"(v) :: "memory");
      lds_signal(L, 8 + lw, tag);
      STAMP(5); STAMPC(7);
      if (!__all(v == tag)) poll_flags((const GAS u64*)(ws + hflag_off), tag);
      __builtin_amdgcn_fence(__ATOMIC_ACQUIRE, "wavefront");
      asm volatile("" ::: "memory");
    }
    STAMP(6);
    v4u av[18][2];
    constexpr int W0 = 4;
#pragma unroll
    for (int j = 0; j < W0; j++) { const unsigned so = hbase + ktf(j) * 1024; av[j][0] = ld_sc1(rws, lane * 16, so); av[j][1] = ld_sc1(rws, lane * 16, so + 112 * 1024); }
    SCHED0();
    acc[0][0] = zero16(); acc[0][1] = zero16(); acc[1][0] = zero16(); acc[1][1] = zero16();
#pragma unroll
    for (int j = 0; j < 18; j++) {
      acc[0][0] = mfma32(wr[j][0], av[j][0], acc[0][0]);
      acc[1][0] = mfma32(wr[j][1], av[j][0], acc[1][0]);
      acc[0][1] = mfma32(wr[j][0], av[j][1], acc[0][1]);
      acc[1][1] = mfma32(wr[j][1], av[j][1], acc[1][1]);
      SCHED0();
#pragma unroll
      for (int n = W0 + 2 * j; n < W0 + 2 * j + 2; n++)
        if (n < 18) { const unsigned so = hbase + ktf(n) * 1024; av[n][0] = ld_sc1(rws, lane * 16, so); av[n][1] = ld_sc1(rws, lane * 16, so + 112 * 1024); }
      SCHED0();
    }
    STAMP(7);
    if (b == 0 && lw == 3) {
      const GAS char* hf = (const GAS char*)ws + WS_HFLAG + ((lane >> 3) * 8 + ((lane & 7) >> 1)) * 128 + (lane & 1) * 16;
      const GAS char* kf = (const GAS char*)ws + WS_KFLAG + (lane >> 4) * 512 + (lane & 15) * 8;
#pragma unroll 1
      for (unsigned spins = 0; spins < SPIN_CAP; ++spins) {
        const u64 v0 = __hip_atomic_load((const GAS u64*)hf, RLX_AGENT), v1 = __hip_atomic_load((const GAS u64*)(hf + 8192), RLX_AGENT);
        const u64 v2 = __hip_atomic_load((const GAS u64*)kf, RLX_AGENT), v3 = __hip_atomic_load((const GAS u64*)(kf + 2048), RLX_AGENT);
        if (__all(v0 == tag && v1 == tag && v2 == tag && v3 == tag)) break;
        __builtin_amdgcn_s_sleep(1);
      }
      if (lane == 0) __hip_atomic_store((GAS u64*)(ws + WS_EPOCH), tag, RLX_AGENT);
    }
    lds_wait(L, 12 + lw, tag);
  } else {
    const int m = lw & 1, nh = lw >> 1, q = lane >> 4;
    tag = load_epoch(ws);
    GAS u64* own_flag;
    if (ffn) {
      int ax0 = m * 8192 + (lane & 15) * 128 + ((q ^ ((lane & 15) >> 1)) * 16);
      int aw[6], as_;
      { const int f = 2 * ((lane >> 3) & 1) + (q & 1), rb = 8 * q + ((lane & 15) >> 2);
        for (int i = 0; i < 6; i++) { const int T = 6 * nh + i; aw[i] = OFF_WM + rb * 384 + (lane & 3) * 8 + (((T & 3) ^ f) * 32) + (T >> 2) * 128; }
        as_ = OFF_WS + rb * 64 + ((nh ^ (q & 1)) * 32) + (lane & 3) * 8; }
      f32x4 ca[7][4];
      for (int i = 0; i < 7; i++) for (int j = 0; j < 4; j++) ca[i][j] = f32x4{0, 0, 0, 0};
      s16x8 xf[2][4], wf[2][7];
      auto rd = [&](int sb, int u, int bi) {
        LAS char* S = L + sb;
#pragma unroll
        for (int t = 0; t < 4; t++) xf[bi][t] = *(LAS s16x8*)(S + (ax0 ^ (u * 64)) + t * 2048);
#pragma unroll
        for (int i = 0; i < 6; i++) wf[bi][i] = trfrag(S + aw[i], (32 * u) * 384, (32 * u + 4) * 384);
        wf[bi][6] = trfrag(S + as_, (32 * u) * 64, (32 * u + 4) * 64);
      };
      auto mm = [&](int bi) {
#pragma unroll
        for (int i = 0; i < 7; i++)
#pragma unroll
          for (int t = 0; t < 4; t++) ca[i][t] = mfma16(wf[bi][i], xf[bi][t], ca[i][t]);
#pragma unroll
        for (int i = 0; i < 18; i++) { __builtin_amdgcn_sched_group_barrier(0x8, 1, 0); __builtin_amdgcn_sched_group_barrier(0x100, 1, 0); }
        __builtin_amdgcn_sched_group_barrier(0x8, 10, 0);
      };
      BAR();
      STAMP(1); STAMPC(0);
      int sb = slot_base(0);
      rd(sb, 0, 0);
#pragma unroll 1
      for (int s = 0; s < 8; s++) {
        rd(sb, 1, 1); mm(0); SCHED0();
        BAR();
        sb = (sb == slot_base(0)) ? slot_base(1) : (sb == slot_base(1) ? slot_base(2) : slot_base(0));
        rd(sb, 0, 0); mm(1); SCHED0();
      }
      STAMP(2); STAMPC(1);
      const int kt_main = 14 * cg + ((cg & 1) ? 2 : 0) + 6 * nh, kt_side = 14 * cg + ((cg & 1) ? 0 : 12) + nh;
#pragma unroll
      for (int i = 0; i < 7; i++) {
        const int kt = (i < 6) ? kt_main + i : kt_side;
#pragma unroll
        for (int a = 0; a < 2; a++) {
          const f32x4 c0 = ca[i][2 * a], c1 = ca[i][2 * a + 1];
          const auto s0 = __builtin_amdgcn_permlane16_swap(pk(c0[0], c0[1]), pk(c1[0], c1[1]), false, false);
          const auto s1 = __builtin_amdgcn_permlane16_swap(pk(c0[2], c0[3]), pk(c1[2], c1[3]), false, false);
          st_sc1(v4u{relu2(s0[0]), relu2(s1[0]), relu2(s0[1]), relu2(s1[1])}, rws, lane * 16, WS_H + ((ti * 4 + m * 2 + a) * 112 + kt) * 1024);
          SCHED0();
        }
      }
      if (HOLD) lds_signal(L, 16 + lw, tag);
      STAMP(3); STAMPC(2);
      own_flag = (GAS u64*)(ws + WS_HFLAG + ((ti * 2 + m) * 4 + (cg >> 1)) * 128 + ((cg & 1) * 2 + nh) * 8);
    } else {
      const int p = nh;
      const int ax0 = m * 8192 + (lane & 15) * 128 + ((q ^ ((lane & 15) >> 1)) * 16);
      int aw[2];
      { const int f = 2 * ((lane >> 3) & 1) + (q & 1), rb = 8 * q + ((lane & 15) >> 2);
        for (int i = 0; i < 2; i++) aw[i] = OFF_WM + rb * 384 + (lane & 3) * 8 + (((2 * p + i) ^ f) * 32); }
      const int ag = GATE_OFF + q * 16;
      v4u idn[2], sel[2];
#pragma unroll
      for (int v = 0; v < 2; v++)
#pragma unroll
        for (int jj = 0; jj < 4; jj++) {
          const int F = 8 * q + 2 * jj, c = 16 * v + (lane & 15);
          idn[v][jj] = ((c == F) ? 0x3f80u : 0u) | ((c == F + 1) ? 0x3f800000u : 0u);
        }
      asm volatile("" : "+v"(idn[0]), "+v"(idn[1]));
      f32x4 cq[2][4], ck[2][4], cv[2][4], cgate[2];
      for (int i = 0; i < 2; i++) { cgate[i] = f32x4{0, 0, 0, 0}; for (int t = 0; t < 4; t++) { cq[i][t] = cgate[i]; ck[i][t] = cgate[i]; cv[i][t] = cgate[i]; } }
      s16x8 xf[2][4], wf[2][6], gf[2];
      const int axp = ax0 + p * 4096, axn = ax0 + (1 - p) * 4096;
      auto rd = [&](int sb, int goff, int u, int bi) {
        LAS char* S = L + sb;
#pragma unroll
        for (int t = 0; t < 4; t++) xf[bi][t] = *(LAS s16x8*)(S + ((t < 2 ? axp : axn) ^ (u * 64)) + (t & 1) * 2048);
#pragma unroll
        for (int i = 0; i < 6; i++) wf[bi][i] = trfrag(S + aw[i & 1], (32 * u) * 384 + (i >> 1) * 128, (32 * u + 4) * 384 + (i >> 1) * 128);
        gf[bi] = *(LAS s16x8*)(L + ag + goff + u * 64);
      };
      auto mm = [&](int bi) {
#pragma unroll
        for (int i = 0; i < 2; i++)
#pragma unroll
          for (int t = 0; t < 4; t++) cq[i][t] = mfma16(wf[bi][i], xf[bi][t], cq[i][t]);
#pragma unroll
        for (int i = 0; i < 2; i++)
#pragma unroll
          for (int t = 0; t < 4; t++) ck[i][t] = mfma16(wf[bi][2 + i], xf[bi][t], ck[i][t]);
#pragma unroll
        for (int i = 0; i < 2; i++)
#pragma unroll
          for (int t = 0; t < 4; t++) cv[i][t] = mfma16(xf[bi][t], wf[bi][4 + i], cv[i][t]);
        cgate[0] = mfma16(gf[bi], xf[bi][0], cgate[0]);
        cgate[1] = mfma16(gf[bi], xf[bi][1], cgate[1]);
#pragma unroll
        for (int i = 0; i < 17; i++) { __builtin_amdgcn_sched_group_barrier(0x8, 1, 0); __builtin_amdgcn_sched_group_barrier(0x100, 1, 0); }
        __builtin_amdgcn_sched_group_barrier(0x8, 9, 0);
      };
      BAR();
      STAMP(1); STAMPC(0);
      int sb = slot_base(0);
      rd(sb, rota * 128, 0, 0);
      v4u wp[2];
      { const int wa = WPHI_OFF + 2 * p * 512 + (8 * hh + ((lane & 15) >> 2)) * 32 + (8 * ((lane >> 4) & 1) + 4 * ((lane & 3) >> 1)) * 2;
        const unsigned sg = ((lane >> 2) & 1) ? 0x80008000u : 0u;
#pragma unroll
        for (int s = 0; s < 2; s++) { wp[s] = __builtin_bit_cast(v4u, trfrag(L + wa, s * 512, s * 512 + 128)); wp[s] ^= sg; }
        asm volatile("" : "+v"(wp[0]), "+v"(wp[1])); }
      SCHED0();
#pragma unroll 1
      for (int s = 0; s < 8; s++) {
        rd(sb, ((s + rota) & 7) * 128, 1, 1); mm(0); SCHED0();
        BAR();
        sb = (sb == slot_base(0)) ? slot_base(1) : (sb == slot_base(1) ? slot_base(2) : slot_base(0));
        rd(sb, ((s + 1 + rota) & 7) * 128, 0, 0); mm(1); SCHED0();
      }
      STAMP(2); STAMPC(1);
      f32x4 kv[2][2], zz[2][2];
      {
        f32x16 P[4]; v4u ph[4][2], vb[4];
#pragma unroll
        for (int u = 0; u < 4; u++) P[u] = mfma32(wp[u & 1], to_frag32(ck[u & 1][u & 2], ck[u & 1][(u & 2) + 1]), zero16());
        SCHED0();
#pragma unroll
        for (int u = 0; u < 4; u++) vb[u] = pack44(cv[u & 1][u & 2], cv[u & 1][(u & 2) + 1]);
        if (lane < 16) { *(LAS float*)(L + SCR_GX + lw * 128 + lane * 4) = cgate[0][0]; *(LAS float*)(L + SCR_GX + lw * 128 + 64 + lane * 4) = cgate[1][0]; }
        SCHED0();
        STAMPC(3);
        softmax16x4(P, ph);
        STAMPC(4);
        const v4u ones = {0x3f803f80u, 0x3f803f80u, 0x3f803f80u, 0x3f803f80u};
        const f32x4 z4 = {0, 0, 0, 0};
        f32x4 Z[4][2][2];
#pragma unroll
        for (int u = 0; u < 4; u++) {
          v4u a0, a1;
#pragma unroll
          for (int i = 0; i < 4; i++) { const auto sw = __builtin_amdgcn_permlane16_swap(ph[u][0][i], ph[u][1][i], false, false); a0[i] = sw[0]; a1[i] = sw[1]; }
#pragma unroll
          for (int v = 0; v < 2; v++) { Z[u][0][v] = mfma16(a0, idn[v], z4); Z[u][1][v] = mfma16(a1, idn[v], z4); }
        }
#pragma unroll
        for (int u = 0; u < 4; u++)
#pragma unroll
          for (int v = 0; v < 2; v++) {
            const v4u a = pack44(Z[u][0][v], Z[u][1][v]);
            kv[u & 1][v] = mfma16(a, vb[u], u < 2 ? z4 : kv[u & 1][v]);
            zz[u & 1][v] = mfma16(a, ones, u < 2 ? z4 : zz[u & 1][v]);
          }
      }
      STAMPC(5);
      const int hd = 4 * cg + 2 * p + m;
      {
        LAS char* xw = L + SCR_XCH + lw * 2176;
        *(LAS f32x4*)(xw + lane * 16) = m ? kv[0][0] : kv[1][0];
        *(LAS f32x4*)(xw + 1024 + lane * 16) = m ? kv[0][1] : kv[1][1];
        if ((lane & 15) == 0) { *(LAS f32x4*)(xw + 2048 + q * 32) = m ? zz[0][0] : zz[1][0]; *(LAS f32x4*)(xw + 2048 + q * 32 + 16) = m ? zz[0][1] : zz[1][1]; }
        lds_signal(L, lw, tag);
        STAMPC(6);
        f32x4 k0 = m ? kv[1][0] : kv[0][0], k1 = m ? kv[1][1] : kv[0][1], y0 = m ? zz[1][0] : zz[0][0], y1 = m ? zz[1][1] : zz[0][1];
        lds_wait(L, lw ^ 1, tag);
        STAMPC(7);
        const LAS char* xr = L + SCR_XCH + (lw ^ 1) * 2176;
        k0 += *(const LAS f32x4*)(xr + lane * 16); k1 += *(const LAS f32x4*)(xr + 1024 + lane * 16);
        y0 += *(const LAS f32x4*)(xr + 2048 + q * 32); y1 += *(const LAS f32x4*)(xr + 2048 + q * 32 + 16);
        st_sc1(pack44(k0, k1), rws, lane * 16, WS_KVP + (hd * 16 + ti) * 1024);
        st_sc1(pack44(y0, y1), rws, (lane & 15) == 0 ? q * 16 : OOB, WS_ZP + (hd * 16 + ti) * 128);
      }
      SCHED0();
      STAMP(3); STAMPC(2);
      v4u pq[4][2];
      {
        f32x16 P[4];
#pragma unroll
        for (int u = 0; u < 4; u++) P[u] = mfma32(wp[u & 1], to_frag32(cq[u & 1][u & 2], cq[u & 1][(u & 2) + 1]), zero16());
#pragma unroll
        for (int u = 0; u < 2; u++)
#pragma unroll
          for (int jj = 0; jj < 4; jj++) {
            const int f0 = 16 * (jj >> 1) + 4 * q + 2 * (jj & 1), rho = lane & 15;
            sel[u][jj] = ((f0 == 16 * u + rho) ? 0x3f80u : 0u) | ((f0 + 1 == 16 * u + rho) ? 0x3f800000u : 0u);
          }
        asm volatile("" : "+v"(sel[0]), "+v"(sel[1]));
        asm volatile("" : "+v"(P[0]), "+v"(P[1]), "+v"(P[2]), "+v"(P[3]));
        WAITV(0);
        if (lane == 0) __hip_atomic_store((GAS u64*)(ws + WS_KFLAG + hd * 128 + ti * 8), tag, RLX_AGENT);
        STAMP(4);
        asm volatile("" : "+v"(P[0]), "+v"(P[1]), "+v"(P[2]), "+v"(P[3]));
        softmax16x4(P, pq);
#pragma unroll
        for (int u = 0; u < 4; u++) asm volatile("" : "+v"(pq[u][0]), "+v"(pq[u][1]));
      }
      lds_wait(L, lw ^ 2, tag);
      float gt[2];
      { const float l0 = *(const LAS float*)(L + SCR_GX + lw * 128 + (lane & 31) * 4), l1 = *(const LAS float*)(L + SCR_GX + (lw ^ 2) * 128 + (lane & 31) * 4);
        gt[0] = __builtin_amdgcn_rcpf(1.f + __builtin_amdgcn_exp2f(-1.4426950408889634f * l0));
        gt[1] = __builtin_amdgcn_rcpf(1.f + __builtin_amdgcn_exp2f(-1.4426950408889634f * l1));
        asm volatile("" : "+v"(gt[0]), "+v"(gt[1])); }
      STAMP(5);
      {
        const int hg = 4 * cg + lw, rho = lane & 15;
        poll_flags((const GAS u64*)(ws + WS_KFLAG + hg * 128 + (lane & 15) * 8), tag);
        STAMP(6);
        const v4u zp = ld_sc1(rws, (lane & 15) * 128 + q * 16, WS_ZP + hg * 2048);
        v4u kp[16];
#pragma unroll
        for (int i = 0; i < 16; i++) kp[i] = ld_sc1(rws, lane * 16, WS_KVP + (hg * 16 + ((i + ti) & 15)) * 1024);
        SCHED0();
        LAS char* ks = L + SCR_KS + lw * 1088;
        float z[8];
#pragma unroll
        for (int jj = 0; jj < 4; jj++) { z[2 * jj] = row_sum16(__uint_as_float(zp[jj] << 16)); z[2 * jj + 1] = row_sum16(__uint_as_float(zp[jj] & 0xffff0000u)); }
        if (rho == 0) {
          LAS char* zr = ks + 1024 + q * 8;
          *(LAS v2u*)(zr) = v2u{pk(z[0], z[1]), pk(z[2], z[3])};
          *(LAS v2u*)(zr + 32) = v2u{pk(z[4], z[5]), pk(z[6], z[7])};
        }
        SCHED0();
        f32x4 t0 = {0, 0, 0, 0}, t1 = {0, 0, 0, 0};
#pragma unroll
        for (int i = 0; i < 16; i++) { t0 = mfma16(sel[0], kp[i], t0); t1 = mfma16(sel[1], kp[i], t1); }
        *(LAS v2u*)(ks + rho * 64 + q * 8) = v2u{pk(t0[0], t0[1]), pk(t0[2], t0[3])};
        *(LAS v2u*)(ks + rho * 64 + 32 + q * 8) = v2u{pk(t1[0], t1[1]), pk(t1[2], t1[3])};
        lds_signal(L, 4 + lw, tag);
      }
      STAMP(7);
      { const int rho = lane & 31, krow = rho < 16 ? (8 * ((rho >> 2) & 1) + (rho & 3) + 4 * (rho >> 3)) : 16;
        lds_wait(L, 4 + (lw ^ 1), tag);
        const LAS char* ks = L + SCR_KS + 2 * p * 1088 + krow * 64 + hh * 32;
        f32x16 o[4];
#pragma unroll
        for (int s = 0; s < 2; s++) {
          const v4u k0 = *(const LAS v4u*)(ks + s * 1088), k1 = *(const LAS v4u*)(ks + s * 1088 + 16);
#pragma unroll
          for (int t = 0; t < 2; t++) { o[2 * t + s] = mfma32(k0, pq[2 * t + s][0], zero16()); o[2 * t + s] = mfma32(k1, pq[2 * t + s][1], o[2 * t + s]); }
        }
#pragma unroll
        for (int u = 0; u < 4; u++) {
          const float sc = gt[u >> 1] * __builtin_amdgcn_rcpf(o[u][8] + 1e-6f);
#pragma unroll
          for (int i = 0; i < 8; i++) o[u][i] *= sc;
          st_sc1(pack8(o[u], 0), rws, lane * 16, WS_O + ((ti * 4 + m * 2 + ((u >> 1) ^ p)) * 32 + 4 * cg + 2 * p + (u & 1)) * 1024);
        }
        const unsigned go = ((cg & 1) == 0 && p == 0) ? (WS_G + ((ti * 2 + m) * 4 + (cg >> 1)) * 256 + lane * 4) : OOB;
        __builtin_amdgcn_raw_buffer_store_b32(__float_as_uint(1.f - (hh ? gt[1] : gt[0])), rws, go, 0, 16);
        own_flag = (GAS u64*)(ws + WS_OFLAG + ((ti * 2 + m) * 4 + (cg >> 1)) * 128 + ((cg & 1) * 2 + p) * 8);
      }
    }
    publish_then_poll(own_flag, (const GAS u64*)(ws + hflag_off), tag, lane);
    STAMP(8);
    v4u av[10][2];
#pragma unroll
    for (int j = 0; j < 10; j++) { const unsigned so = hbase + ktf(18 + j) * 1024; av[j][0] = ld_sc1(rws, lane * 16, so); av[j][1] = ld_sc1(rws, lane * 16, so + 112 * 1024); }
    SCHED0();
    lds_wait(L, 8 + lw, tag);
    acc[0][0] = zero16(); acc[0][1] = zero16(); acc[1][0] = zero16(); acc[1][1] = zero16();
    auto zoff = [&](int j) { return j < 3 ? zb1 + j * 2048 : (j < 9 ? zb2 + (j - 3) * 2048 : (j < 14 ? zb3 + (j - 9) * 2048 : zb4 + (j - 14) * 2048)); };
#pragma unroll
    for (int j = 0; j < 10; j++) {
      const s16x8 w0 = trfrag(L + zoff(j) + awB0, 0, 512), w1f = trfrag(L + zoff(j) + awB1, 0, 512);
      acc[0][0] = mfma32(w0, av[j][0], acc[0][0]);
      acc[1][0] = mfma32(w1f, av[j][0], acc[1][0]);
      acc[0][1] = mfma32(w0, av[j][1], acc[0][1]);
      acc[1][1] = mfma32(w1f, av[j][1], acc[1][1]);
    }
    SCHED0();
    lds_signal(L, 12 + lw, tag);
    STAMP(9);
    s16x8 wa[8][2];
#pragma unroll
    for (int j = 0; j < 8; j++) { wa[j][0] = trfrag(L + zoff(10 + j) + awB0, 0, 512); wa[j][1] = trfrag(L + zoff(10 + j) + awB1, 0, 512); }
    SCHED0();
    poll_flags((const GAS u64*)(ws + hflag_off + (WS_OFLAG - WS_HFLAG)), tag);
    STAMP(10);
    v4u ov[8][2];
    const unsigned go = WS_G + (t64 * 4 + lw) * 256;
    const float g0 = __uint_as_float(__builtin_amdgcn_raw_buffer_load_b32(rws, (lane & 31) * 4, go, 16));
    const float g1 = __uint_as_float(__builtin_amdgcn_raw_buffer_load_b32(rws, (lane & 31) * 4, go + 128, 16));
    const unsigned obase = WS_O + (t64 * 2 * 32 + 8 * lw) * 1024;
#pragma unroll
    for (int j = 0; j < 8; j++) { const unsigned so = obase + ((j + rotc) & 7) * 1024; ov[j][0] = ld_sc1(rws, lane * 16, so); ov[j][1] = ld_sc1(rws, lane * 16, so + 32 * 1024); }
    SCHED0();
    if (lw == 0) *(LAS float*)(L + SCR_G2 + lane * 4) = hh ? g1 : g0;
#pragma unroll
    for (int i = 0; i < 16; i++) { acc[0][0][i] *= g0; acc[1][0][i] *= g0; acc[0][1][i] *= g1; acc[1][1][i] *= g1; }
    SCHED0();
#pragma unroll
    for (int j = 0; j < 8; j++) {
      acc[0][0] = mfma32(wa[j][0], ov[j][0], acc[0][0]);
      acc[1][0] = mfma32(wa[j][1], ov[j][0], acc[1][0]);
      acc[0][1] = mfma32(wa[j][0], ov[j][1], acc[0][1]);
      acc[1][1] = mfma32(wa[j][1], ov[j][1], acc[1][1]);
    }
  }
  STAMP(11);
  {
    const int half = wid >> 2;
    const int pa = half ? zb2 : zb3, pb = half ? zb2 + 8192 : zb4, pc = half ? zb1 : zb4 + 4096;
    LAS char* wl = L + lane * 16;
#pragma unroll
    for (int d = 0; d < 4; d++) {
      const f32x16& t = acc[d >> 1][d & 1];
      LAS char* w = wl + (d < 2 ? pa + d * 4096 : (d == 2 ? pb : pc));
#pragma unroll
      for (int i = 0; i < 4; i++) *(LAS f32x4*)(w + i * 1024) = f32x4{t[4 * i], t[4 * i + 1], t[4 * i + 2], t[4 * i + 3]};
    }
    BAR();
    STAMP(12);
    const float g = *(const LAS float*)(L + SCR_G2 + (lw & 1) * 128 + (lane & 31) * 4);
    const LAS char* rc = L + lane * 16 + half * 2048 + (lw < 2 ? lw * 4096 : 20 * 2048 + (lw - 2) * 4096);
    const LAS char* rl = L + lane * 16 + half * 2048 + (lw < 3 ? 56 * 2048 + lw * 4096 : 44 * 2048);
    const int sc = lw < 2 ? 5 * 2048 : 4 * 2048, sl = lw < 3 ? 6 * 2048 : 3 * 2048;
    f32x4 c0 = *(const LAS f32x4*)rc, c1 = *(const LAS f32x4*)(rc + 1024), l0 = *(const LAS f32x4*)rl, l1 = *(const LAS f32x4*)(rl + 1024);
#pragma unroll
    for (int i = 1; i < 4; i++) {
      c0 += *(const LAS f32x4*)(rc + i * sc); c1 += *(const LAS f32x4*)(rc + i * sc + 1024);
      l0 += *(const LAS f32x4*)(rl + i * sl); l1 += *(const LAS f32x4*)(rl + i * sl + 1024);
    }
    c0 += l0 * g; c1 += l1 * g;
    const unsigned a0 = pk(c0[0], c0[1]), a1 = pk(c0[2], c0[3]), b0 = pk(c1[0], c1[1]), b1 = pk(c1[2], c1[3]);
    const auto r0 = __builtin_amdgcn_permlane32_swap(a0, b0, false, false);
    const auto r1 = __builtin_amdgcn_permlane32_swap(a1, b1, false, false);
    const int tok = t64 * 64 + (lw & 1) * 32 + (lane & 31);
    const int col = jc * 64 + (lw >> 1) * 32 + half * 16 + hh * 8;
    const rsrc_t ro = __builtin_amdgcn_make_buffer_rsrc((void*)out, (short)0, 2048 * 512 * 2, 0x00020000);
    __builtin_amdgcn_raw_buffer_store_b128(v4u{r0[0], r1[0], r0[1], r1[1]}, ro, (tok * 512 + col) * 2, 0, OUTSC);
  }
  STAMP(13);
  STAMP_FLUSH;
}

#define DIAG_LAUNCH

extern "C" void kernel_launch(void* const* d_in, const int* in_sizes, int n_in, void* d_out, int out_size, void* d_ws, size_t ws_size,
                              hipStream_t stream) {
  (void)hipFuncSetAttribute((const void*)fused_kernel, hipFuncAttributeMaxDynamicSharedMemorySize, LDS_BYTES);
  fused_kernel<<<256, 512, LDS_BYTES, stream>>>((const u16*)d_in[0], (const u16*)d_in[1], (const u16*)d_in[2], (const u16*)d_in[3],
                                                (const u16*)d_in[5], (const u16*)d_in[6], (const u16*)d_in[7], (u16*)d_out, (char*)d_ws);
  DIAG_LAUNCH
}
```

```cpp
#include <hip/hip_runtime.h>
#include <hip/hip_bf16.h>

typedef unsigned short u16;
typedef unsigned long long u64;
typedef unsigned v4u __attribute__((ext_vector_type(4)));
typedef unsigned v2u __attribute__((ext_vector_type(2)));
typedef short s16x4 __attribute__((ext_vector_type(4)));
typedef short s16x8 __attribute__((ext_vector_type(8)));
typedef __bf16 bf16x8 __attribute__((ext_vector_type(8)));
typedef __bf16 bf16x2 __attribute__((ext_vector_type(2)));
typedef float f32x2 __attribute__((ext_vector_type(2)));
typedef float f32x4 __attribute__((ext_vector_type(4)));
typedef float f32x16 __attribute__((ext_vector_type(16)));
typedef __amdgpu_buffer_rsrc_t rsrc_t;
#define LAS __attribute__((address_space(3)))
#define GAS __attribute__((address_space(1)))
#define DI __device__ __forceinline__
#define RLX_AGENT __ATOMIC_RELAXED, __HIP_MEMORY_SCOPE_AGENT

#define DIAG 0
#define PFW 1
#define HOLD 1
#define PFC 1
#define PFT 1
#define ROTA 0
#define ROTB 1
#define MAPB 0
#define OUTSC 0

constexpr unsigned WS_EPOCH = 0;
constexpr unsigned WS_HFLAG = 4096;
constexpr unsigned WS_OFLAG = WS_HFLAG + 128 * 128;
constexpr unsigned WS_KFLAG = WS_OFLAG + 128 * 128;
constexpr unsigned WS_G     = WS_KFLAG + 32 * 128;
constexpr unsigned WS_ZP    = WS_G + 128 * 256;
constexpr unsigned WS_KVP   = WS_ZP + 512 * 128;
constexpr unsigned WS_O     = WS_KVP + 512 * 1024;
constexpr unsigned OST = 33 * 1024, HST = 113 * 1024;
constexpr unsigned WS_H     = WS_O + 64 * OST;
constexpr unsigned WS_END   = WS_H + 64 * HST;
constexpr unsigned WS_DIAG  = WS_END;
constexpr unsigned OOB = 0x7ffffff0u;

constexpr int SLOT = 45056, OFF_WM = 16384, OFF_WS = 40960;
constexpr int LDS_BYTES = 163840;
__host__ __device__ constexpr int slot_base(int i) { return i == 0 ? 0 : (i == 1 ? SLOT : 58 * 2048); }
constexpr int GATE_OFF = OFF_WS;
constexpr int WPHI_OFF = OFF_WS + 1024;
constexpr int PF_OFF = 56 * 2048;
constexpr int SCR = 36 * 2048;
constexpr int SCR_XCH = SCR;
constexpr int SCR_KS = SCR + 8704;
constexpr int SCR_GX = SCR_KS + 4352;
constexpr int SCR_FLAG = SCR_GX + 512;
constexpr int SCR_G2 = SCR_FLAG + 256;
constexpr unsigned SPIN_CAP = 1u << 16;

DI void dma(unsigned lds, int imm, unsigned voff, rsrc_t r, unsigned soff) {
  asm volatile("s_add_i32 m0, %0, %4\n\ts_nop 0\n\tbuffer_load_dwordx4 %1, %2, %3 offen lds" ::"s"(lds), "v"(voff), "s"(r), "s"(soff), "i"(imm) : "memory", "scc");
}
#define OPAQUE_S(x) asm volatile("" : "+s"(x))
DI s16x8 trfrag(LAS char* p, int off0, int off1) {
  s16x4 a = __builtin_amdgcn_ds_read_tr16_b64_v4i16((LAS s16x4*)(p + off0));
  s16x4 b = __builtin_amdgcn_ds_read_tr16_b64_v4i16((LAS s16x4*)(p + off1));
  return __builtin_shufflevector(a, b, 0, 1, 2, 3, 4, 5, 6, 7);
}
template <class A, class B> DI f32x4 mfma16(A a, B b, f32x4 c) {
  return __builtin_amdgcn_mfma_f32_16x16x32_bf16(__builtin_bit_cast(bf16x8, a), __builtin_bit_cast(bf16x8, b), c, 0, 0, 0);
}
template <class A, class B> DI f32x16 mfma32(A a, B b, f32x16 c) {
  return __builtin_amdgcn_mfma_f32_32x32x16_bf16(__builtin_bit_cast(bf16x8, a), __builtin_bit_cast(bf16x8, b), c, 0, 0, 0);
}
DI unsigned pk(float a, float b) { f32x2 v = {a, b}; return __builtin_bit_cast(unsigned, __builtin_convertvector(v, bf16x2)); }
typedef short s16x2 __attribute__((ext_vector_type(2)));
DI unsigned relu2(unsigned v) { return __builtin_bit_cast(unsigned, __builtin_elementwise_max(__builtin_bit_cast(s16x2, v), s16x2{0, 0})); }
DI v4u pack44(const f32x4& a, const f32x4& b) { return v4u{pk(a[0], a[1]), pk(a[2], a[3]), pk(b[0], b[1]), pk(b[2], b[3])}; }
DI v4u to_frag32(const f32x4& c0, const f32x4& c1) {
  const auto s0 = __builtin_amdgcn_permlane16_swap(pk(c0[0], c0[1]), pk(c1[0], c1[1]), false, false);
  const auto s1 = __builtin_amdgcn_permlane16_swap(pk(c0[2], c0[3]), pk(c1[2], c1[3]), false, false);
  return v4u{s0[0], s1[0], s0[1], s1[1]};
}
DI v4u pack8(const f32x16& x, int s) {
  return v4u{pk(x[8 * s], x[8 * s + 1]), pk(x[8 * s + 2], x[8 * s + 3]), pk(x[8 * s + 4], x[8 * s + 5]), pk(x[8 * s + 6], x[8 * s + 7])};
}
DI f32x16 zero16() { f32x16 z; for (int i = 0; i < 16; i++) z[i] = 0.f; return z; }
#define WAITV(n) asm volatile("s_waitcnt vmcnt(" #n ")" ::: "memory")
#define WAITL() asm volatile("s_waitcnt lgkmcnt(0)" ::: "memory")
#define BAR() do { asm volatile("s_waitcnt lgkmcnt(0)" ::: "memory"); __builtin_amdgcn_s_barrier(); asm volatile("" ::: "memory"); } while (0)
#define SCHED0() __builtin_amdgcn_sched_barrier(0)
DI v4u ld_sc1(rsrc_t r, unsigned voff, unsigned soff) { return __builtin_amdgcn_raw_buffer_load_b128(r, voff, soff, 16); }
DI void st_sc1(v4u v, rsrc_t r, unsigned voff, unsigned soff) { __builtin_amdgcn_raw_buffer_store_b128(v, r, voff, soff, 16); }

DI void poll_flags(const GAS u64* p, u64 tag) {
#pragma unroll 1
  for (unsigned spins = 0; spins < SPIN_CAP; ++spins) {
    u64 v = __hip_atomic_load(p, RLX_AGENT);
    if (__all(v == tag)) break;
    __builtin_amdgcn_s_sleep(1);
  }
  __builtin_amdgcn_fence(__ATOMIC_ACQUIRE, "wavefront");
  asm volatile("" ::: "memory");
}
DI void publish_then_poll(GAS u64* own, const GAS u64* p, u64 tag, int lane) {
  u64 v = __hip_atomic_load(p, RLX_AGENT);
  asm volatile("s_waitcnt vmcnt(0)" : "+v"(v) :: "memory");
  if (lane == 0) __hip_atomic_store(own, tag, RLX_AGENT);
  if (!__all(v == tag)) poll_flags(p, tag);
  __builtin_amdgcn_fence(__ATOMIC_ACQUIRE, "wavefront");
  asm volatile("" ::: "memory");
}
DI void touch(unsigned lds, unsigned voff, rsrc_t r) {
  asm volatile("s_mov_b32 m0, %0\n\ts_nop 0\n\tbuffer_load_dword %1, %2, 0 offen lds" ::"s"(lds), "v"(voff), "s"(r) : "memory");
}
DI u64 load_epoch(const char* ws) {
  u64 e;
  asm volatile("s_load_dwordx2 %0, %1, 0x0\n\ts_waitcnt lgkmcnt(0)" : "=s"(e) : "s"(ws) : "memory");
  return e + 1;
}
DI float row_sum16(float x) {
  x += __uint_as_float(__builtin_amdgcn_mov_dpp(__float_as_uint(x), 0x128, 0xf, 0xf, true));
  x += __uint_as_float(__builtin_amdgcn_mov_dpp(__float_as_uint(x), 0x124, 0xf, 0xf, true));
  x += __uint_as_float(__builtin_amdgcn_mov_dpp(__float_as_uint(x), 0x122, 0xf, 0xf, true));
  x += __uint_as_float(__builtin_amdgcn_mov_dpp(__float_as_uint(x), 0x121, 0xf, 0xf, true));
  return x;
}
DI void lds_wait(LAS char* L, int idx, u64 tag) {
  volatile LAS u64* f = (volatile LAS u64*)(L + SCR_FLAG + idx * 8);
#pragma unroll 1
  for (unsigned spins = 0; spins < (SPIN_CAP << 4); ++spins) {
    u64 v = *f;
    if (__all(v == tag)) break;
    __builtin_amdgcn_s_sleep(1);
  }
  asm volatile("" ::: "memory");
}
DI void lds_signal(LAS char* L, int idx, u64 tag) {
  asm volatile("s_waitcnt lgkmcnt(0)" ::: "memory");
  *(volatile LAS u64*)(L + SCR_FLAG + idx * 8) = tag;
}
#define STAMP(i) do {} while (0)
#define STAMPC(i) do {} while (0)
#define STAMP_DECL
#define STAMP_FLUSH

DI void softmax16x4(f32x16 (&p)[4], v4u (&o)[4][2]) {
  const float L2E = 1.4426950408889634f;
  float m[4], s0[4], s1[4];
#pragma unroll
  for (int u = 0; u < 4; u++) m[u] = fmaxf(fmaxf(p[u][0], p[u][1]), p[u][2]);
#pragma unroll
  for (int i = 3; i < 15; i += 2)
#pragma unroll
    for (int u = 0; u < 4; u++) m[u] = fmaxf(fmaxf(m[u], p[u][i]), p[u][i + 1]);
#pragma unroll
  for (int u = 0; u < 4; u++) m[u] = -L2E * fmaxf(m[u], p[u][15]);
#pragma unroll
  for (int i = 0; i < 16; i++)
#pragma unroll
    for (int u = 0; u < 4; u++) p[u][i] = __builtin_amdgcn_exp2f(__builtin_fmaf(p[u][i], L2E, m[u]));
#pragma unroll
  for (int u = 0; u < 4; u++) { s0[u] = p[u][0]; s1[u] = p[u][1]; }
#pragma unroll
  for (int i = 2; i < 16; i += 2)
#pragma unroll
    for (int u = 0; u < 4; u++) { s0[u] += p[u][i]; s1[u] += p[u][i + 1]; }
#pragma unroll
  for (int u = 0; u < 4; u++) s0[u] = __builtin_amdgcn_rcpf(s0[u] + s1[u]);
#pragma unroll
  for (int u = 0; u < 4; u++) {
#pragma unroll
    for (int i = 0; i < 16; i++) p[u][i] *= s0[u];
    o[u][0] = pack8(p[u], 0); o[u][1] = pack8(p[u], 1);
  }
}

__global__ __launch_bounds__(512, 2) void fused_kernel(const u16* __restrict__ x, const u16* __restrict__ wqkv, const u16* __restrict__ wphi,
                                                       const u16* __restrict__ wo, const u16* __restrict__ w1, const u16* __restrict__ w2,
                                                       const u16* __restrict__ gw, u16* __restrict__ out, char* __restrict__ ws) {
  extern __shared__ __attribute__((aligned(16))) char shm[];
  LAS char* L = (LAS char*)shm;
  const unsigned ldsbase = (unsigned)(size_t)L;
  const int tid = threadIdx.x, lane = tid & 63;
  const int wid = __builtin_amdgcn_readfirstlane(tid >> 6);
  const int lw = wid & 3;
  const int b = blockIdx.x, xcd = b & 7, r = b >> 3;
  const bool ffn = (r >> 4) == 0;
  const int ti = (xcd & 1) * 8 + (r & 7), cg = (xcd >> 1) * 2 + ((r >> 3) & 1);
  const int t64 = MAPB ? (xcd >> 1) * 8 + (r & 7) : xcd * 4 + (r & 3);
  const int jc = MAPB ? (xcd & 1) * 4 + (r >> 3) : r >> 2;
  const int rota = ROTA ? ((((r & 7) + 2 * (r >> 3)) & (ROTA - 1)) * (8 / (ROTA ? ROTA : 1))) : 0;
  int rotb = ROTB ? (MAPB ? (7 * (r >> 3) + ((7 * (r & 7)) >> 1)) % 28 : (7 * (r & 3) + ((7 * (r >> 2)) >> 1)) % 28) : 0;
  const int rotc = ROTB ? (MAPB ? (2 * (r >> 3) + (r & 7)) & 7 : (2 * (r & 3) + (r >> 2)) & 7) : 0;
  const rsrc_t rws = __builtin_amdgcn_make_buffer_rsrc((void*)ws, (short)0, WS_END, 0x00020000);
#define RX __builtin_amdgcn_make_buffer_rsrc((void*)x, (short)0, 2048 * 512 * 2, 0x00020000)
#define RW1 __builtin_amdgcn_make_buffer_rsrc((void*)w1, (short)0, 512 * 1792 * 2, 0x00020000)
#define RWQ __builtin_amdgcn_make_buffer_rsrc((void*)wqkv, (short)0, 512 * 1536 * 2, 0x00020000)
#define RW2 __builtin_amdgcn_make_buffer_rsrc((void*)w2, (short)0, 1792 * 512 * 2, 0x00020000)
#define RWO __builtin_amdgcn_make_buffer_rsrc((void*)wo, (short)0, 512 * 512 * 2, 0x00020000)
  u64 pc0 = 0;
  if (PFC) asm volatile("s_getpc_b64 %0" : "=s"(pc0));
  STAMP_DECL
  STAMP(0);
  u64 tag;
  f32x16 acc[2][2];
  const int hh = lane >> 5;
  const int fB = 2 * ((lane >> 3) & 1) + hh;
  const int awB0 = (8 * hh + ((lane & 15) >> 2)) * 128 + ((((lane >> 4) & 1) ^ fB) * 32) + (lane & 3) * 8;
  const int awB1 = awB0 ^ 64;
  const int zb1 = (44 + 3 * lw) * 2048, zb2 = (56 + 6 * lw) * 2048, zb3 = (5 * lw) * 2048, zb4 = (20 + 4 * lw) * 2048;
  const unsigned hflag_off = WS_HFLAG + (t64 * 4 + lw) * 128 + (lane & 3) * 8;
  const unsigned hbase = WS_H + t64 * 2 * HST + 28 * lw * 1024;
  auto ktf = [&](int j) { if (ROTB) OPAQUE_S(rotb); const int k = j + rotb; return k >= 28 ? k - 28 : k; };

  if (wid >= 4) {
    const rsrc_t rx = RX, rw2 = RW2, rwo = RWO;
    const rsrc_t rw = __builtin_amdgcn_make_buffer_rsrc((void*)(ffn ? w1 : wqkv), (short)0, ffn ? 512 * 1792 * 2 : 512 * 1536 * 2, 0x00020000);
    const int RS = ffn ? 3584 : 3072, LSTR = ffn ? 128 : 1024;
    const int maincol = ffn ? (cg * 448 + (cg & 1) * 64) : cg * 128;
    const int sidecol = cg * 448 + ((cg & 1) ? 0 : 384);
    unsigned vx[2], vw[6], vs, vb[2];
    for (int p = 0; p < 2; p++) vx[p] = (lane >> 3) * 1024 + (((lane & 7) ^ (4 * p + (lane >> 4))) * 16);
    { const int row = lane >> 2; vs = row * 3584 + (((lane & 3) ^ (((row >> 3) & 1) << 1)) * 16); }
    const unsigned lb1 = ldsbase + lw * 1024, lb4 = ldsbase + lw * 4096, lb6 = ldsbase + lw * 6144;
    unsigned sx = ti * 131072 + lw * 32768, sw = 16 * lw * RS + maincol, ss = 16 * lw * 3584 + sidecol;
    auto issue_x = [&](int s, int sl) {
      const int sr = (s + rota) & 7;
      OPAQUE_S(sx); OPAQUE_S(ss);
      if (ffn) dma(lb1, sl + OFF_WS, vs, rw, ss + sr * 64 * 3584);
#pragma unroll
      for (int i = 0; i < 4; i++) dma(lb4, sl + i * 1024, vx[i & 1], rx, sx + i * 8192 + sr * 128);
    };
    auto issue_w = [&](int s, int sl) {
      const int sr = (s + rota) & 7;
      OPAQUE_S(sw);
      const unsigned so = sw + sr * 64 * RS;
#pragma unroll
      for (int e = 0; e < 6; e++) dma(lb6, sl + OFF_WM + e * 1024, vw[e], rw, so);
    };
    auto issue = [&](int s, int sl) { issue_x(s, sl); issue_w(s, sl); };
    if (!ffn && lw < 3) {
      const rsrc_t rt = __builtin_amdgcn_make_buffer_rsrc((void*)(lw == 0 ? gw : wphi), (short)0, lw == 0 ? 1024 : 32 * 256 * 2, 0x00020000);
      dma(lb1, GATE_OFF, lane * 16, rt, lw ? cg * 2048 + (lw - 1) * 1024 : 0);
    }
    SCHED0();
    issue_x(0, slot_base(0));
    SCHED0();
    for (int e = 0; e < 6; e++) {
      const int n = 64 * e + lane, row = n / 24, cp = n % 24, f = 2 * ((row >> 1) & 1) + ((row >> 3) & 1);
      vw[e] = row * RS + (cp >> 3) * LSTR + (((cp & 7) ^ (f << 1)) * 16);
    }
    issue_w(0, slot_base(0));
    issue(1, slot_base(1));
    for (int e = 0; e < 2; e++) vb[e] = (8 * e + (lane >> 3)) * 1024 + (((lane & 7) ^ ((2 * ((lane >> 4) & 1) + e) << 1)) * 16);
    const unsigned s2 = 28 * lw * 16384 + jc * 128, so8 = 8 * lw * 16384 + jc * 128;
    auto wblk_own = [&](int j, int zb, int zo) {
      const unsigned so = s2 + ktf(j) * 16384;
      dma(ldsbase + zb, zo, vb[0], rw2, so); dma(ldsbase + zb, zo + 1024, vb[1], rw2, so);
    };
    auto wblk_c = [&](int j, int zb, int zo) {
      if (j < 10) { const unsigned so = s2 + ktf(18 + j) * 16384; dma(ldsbase + zb, zo, vb[0], rw2, so); dma(ldsbase + zb, zo + 1024, vb[1], rw2, so); }
      else { const unsigned so = so8 + ((j - 10 + rotc) & 7) * 16384; dma(ldsbase + zb, zo, vb[0], rwo, so); dma(ldsbase + zb, zo + 1024, vb[1], rwo, so); }
    };
    s16x8 wr[18][2];
    auto wread = [&](int j, int zoff) { wr[j][0] = trfrag(L + zoff + awB0, 0, 512); wr[j][1] = trfrag(L + zoff + awB1, 0, 512); };

    tag = load_epoch(ws);
    WAITV(10); BAR();
    STAMP(1);
    const unsigned pl = ldsbase + PF_OFF + lw * 256;
    auto tw = [&](int i) {
      if (!PFW) return;
      if (i < 4) {
        const int nl = MAPB ? 14 : 28, it = ((i * 32 + r) * 4 + lw) * nl + lane;
        touch(pl, lane < nl ? (MAPB ? (it >> 2) * 1024 + ((xcd & 1) * 4 + (it & 3)) * 128 : it * 128) : OOB, rw2);
      } else {
        const int nl = MAPB ? 16 : 32, it = (r * 4 + lw) * nl + lane;
        touch(pl, lane < nl ? (MAPB ? (it >> 2) * 1024 + ((xcd & 1) * 4 + (it & 3)) * 128 : it * 128) : OOB, rwo);
      }
    };
    issue(2, slot_base(2)); tw(0);
#pragma unroll
    for (int s = 0; s < 5; s++) {
      if (!PFW) WAITV(10); else if (s == 0) WAITV(11); else WAITV(12);
      BAR(); issue(s + 3, slot_base(s % 3));
      if (s < 4) tw(s + 1);
    }
#pragma unroll
    for (int j = 0; j < 3; j++) wblk_own(j, zb1, j * 2048);
    WAITV(16); BAR();
#pragma unroll
    for (int j = 0; j < 6; j++) wblk_own(3 + j, zb2, j * 2048);
    WAITV(18); BAR();
#pragma unroll
    for (int j = 0; j < 5; j++) wblk_own(9 + j, zb3, j * 2048);
    BAR();
    STAMP(2); STAMPC(0);
#pragma unroll
    for (int j = 0; j < 4; j++) wblk_own(14 + j, zb4, j * 2048);
    WAITV(18);
    STAMPC(1);
#pragma unroll
    for (int j = 0; j < 3; j++) wread(j, zb1 + j * 2048);
#pragma unroll
    for (int j = 0; j < 6; j++) wread(3 + j, zb2 + j * 2048);
    WAITL(); SCHED0();
    if (HOLD && ffn) lds_wait(L, 16 + lw, tag);
    STAMPC(2);
#pragma unroll
    for (int j = 0; j < 3; j++) wblk_c(j, zb1, j * 2048);
#pragma unroll
    for (int j = 0; j < 6; j++) wblk_c(3 + j, zb2, j * 2048);
    STAMPC(3);
    WAITV(18);
    STAMPC(4);
#pragma unroll
    for (int j = 0; j < 5; j++) wread(9 + j, zb3 + j * 2048);
#pragma unroll
    for (int j = 0; j < 4; j++) wread(14 + j, zb4 + j * 2048);
    WAITL(); SCHED0();
    STAMPC(5);
#pragma unroll
    for (int j = 0; j < 5; j++) wblk_c(9 + j, zb3, j * 2048);
#pragma unroll
    for (int j = 0; j < 4; j++) wblk_c(14 + j, zb4, j * 2048);
    STAMP(3); STAMPC(6);
    {
      u64 v = __hip_atomic_load((const GAS u64*)(ws + hflag_off), RLX_AGENT);
      asm volatile("s_waitcnt vmcnt(0)" : "+v"(v) :: "memory");
      lds_signal(L, 8 + lw, tag);
      STAMP(5); STAMPC(7);
      if (!__all(v == tag)) poll_flags((const GAS u64*)(ws + hflag_off), tag);
      __builtin_amdgcn_fence(__ATOMIC_ACQUIRE, "wavefront");
      asm volatile("" ::: "memory");
    }
    STAMP(6);
    v4u av[18][2];
    constexpr int W0 = 4;
#pragma unroll
    for (int j = 0; j < W0; j++) { const unsigned so = hbase + ktf(j) * 1024; av[j][0] = ld_sc1(rws, lane * 16, so); av[j][1] = ld_sc1(rws, lane * 16, so + HST); }
    SCHED0();
    acc[0][0] = zero16(); acc[0][1] = zero16(); acc[1][0] = zero16(); acc[1][1] = zero16();
#pragma unroll
    for (int j = 0; j < 18; j++) {
      acc[0][0] = mfma32(wr[j][0], av[j][0], acc[0][0]);
      acc[1][0] = mfma32(wr[j][1], av[j][0], acc[1][0]);
      acc[0][1] = mfma32(wr[j][0], av[j][1], acc[0][1]);
      acc[1][1] = mfma32(wr[j][1], av[j][1], acc[1][1]);
      SCHED0();
#pragma unroll
      for (int n = W0 + 2 * j; n < W0 + 2 * j + 2; n++)
        if (n < 18) { const unsigned so = hbase + ktf(n) * 1024; av[n][0] = ld_sc1(rws, lane * 16, so); av[n][1] = ld_sc1(rws, lane * 16, so + HST); }
      SCHED0();
    }
    STAMP(7);
    if (b == 0 && lw == 3) {
      const GAS char* hf = (const GAS char*)ws + WS_HFLAG + ((lane >> 3) * 8 + ((lane & 7) >> 1)) * 128 + (lane & 1) * 16;
      const GAS char* kf = (const GAS char*)ws + WS_KFLAG + (lane >> 4) * 512 + (lane & 15) * 8;
#pragma unroll 1
      for (unsigned spins = 0; spins < SPIN_CAP; ++spins) {
        const u64 v0 = __hip_atomic_load((const GAS u64*)hf, RLX_AGENT), v1 = __hip_atomic_load((const GAS u64*)(hf + 8192), RLX_AGENT);
        const u64 v2 = __hip_atomic_load((const GAS u64*)kf, RLX_AGENT), v3 = __hip_atomic_load((const GAS u64*)(kf + 2048), RLX_AGENT);
        if (__all(v0 == tag && v1 == tag && v2 == tag && v3 == tag)) break;
        __builtin_amdgcn_s_sleep(1);
      }
      if (lane == 0) __hip_atomic_store((GAS u64*)(ws + WS_EPOCH), tag, RLX_AGENT);
    }
    lds_wait(L, 12 + lw, tag);
  } else {
    const int m = lw & 1, nh = lw >> 1, q = lane >> 4;
    if (PFC && lw == 0) {
      const rsrc_t rc = __builtin_amdgcn_make_buffer_rsrc((void*)(pc0 & ~255ull), (short)0, 31 * 1024, 0x00020000);
      touch(ldsbase + PF_OFF + 1024, lane < 8 ? (r * 8 + lane) * 128 : OOB, rc);
    }
    if (PFT && lw == 1) {
      const int row = lane < 4 ? ti * 4 + lane : t64 * 2 + (lane & 1);
      const unsigned o = lane < 6 ? WS_H + row * HST + 112 * 1024 : (lane < 12 ? WS_O + (lane < 10 ? ti * 4 + (lane - 6) : t64 * 2 + (lane & 1)) * OST + 32 * 1024 : (lane == 12 ? 2048 : OOB));
      touch(ldsbase + PF_OFF + 1280, o, rws);
      touch(ldsbase + PF_OFF + 1280, lane == 0 ? (t64 * 64 * 512 + jc * 64) * 2 : OOB, __builtin_amdgcn_make_buffer_rsrc((void*)out, (short)0, 2048 * 512 * 2, 0x00020000));
    }
    tag = load_epoch(ws);
    GAS u64* own_flag;
    if (ffn) {
      int ax0 = m * 8192 + (lane & 15) * 128 + ((q ^ ((lane & 15) >> 1)) * 16);
      int aw[6], as_;
      { const int f = 2 * ((lane >> 3) & 1) + (q & 1), rb = 8 * q + ((lane & 15) >> 2);
        for (int i = 0; i < 6; i++) { const int T = 6 * nh + i; aw[i] = OFF_WM + rb * 384 + (lane & 3) * 8 + (((T & 3) ^ f) * 32) + (T >> 2) * 128; }
        as_ = OFF_WS + rb * 64 + ((nh ^ (q & 1)) * 32) + (lane & 3) * 8; }
      f32x4 ca[7][4];
      for (int i = 0; i < 7; i++) for (int j = 0; j < 4; j++) ca[i][j] = f32x4{0, 0, 0, 0};
      s16x8 xf[2][4], wf[2][7];
      auto rd = [&](int sb, int u, int bi) {
        LAS char* S = L + sb;
#pragma unroll
        for (int t = 0; t < 4; t++) xf[bi][t] = *(LAS s16x8*)(S + (ax0 ^ (u * 64)) + t * 2048);
#pragma unroll
        for (int i = 0; i < 6; i++) wf[bi][i] = trfrag(S + aw[i], (32 * u) * 384, (32 * u + 4) * 384);
        wf[bi][6] = trfrag(S + as_, (32 * u) * 64, (32 * u + 4) * 64);
      };
      auto mm = [&](int bi) {
#pragma unroll
        for (int i = 0; i < 7; i++)
#pragma unroll
          for (int t = 0; t < 4; t++) ca[i][t] = mfma16(wf[bi][i], xf[bi][t], ca[i][t]);
#pragma unroll
        for (int i = 0; i < 18; i++) { __builtin_amdgcn_sched_group_barrier(0x8, 1, 0); __builtin_amdgcn_sched_group_barrier(0x100, 1, 0); }
        __builtin_amdgcn_sched_group_barrier(0x8, 10, 0);
      };
      BAR();
      STAMP(1); STAMPC(0);
      int sb = slot_base(0);
      rd(sb, 0, 0);
#pragma unroll 1
      for (int s = 0; s < 8; s++) {
        rd(sb, 1, 1); mm(0); SCHED0();
        if ((PFC | PFT) && s == 3) WAITV(0);
        BAR();
        sb = (sb == slot_base(0)) ? slot_base(1) : (sb == slot_base(1) ? slot_base(2) : slot_base(0));
        rd(sb, 0, 0); mm(1); SCHED0();
      }
      STAMP(2); STAMPC(1);
      const int kt_main = 14 * cg + ((cg & 1) ? 2 : 0) + 6 * nh, kt_side = 14 * cg + ((cg & 1) ? 0 : 12) + nh;
#pragma unroll
      for (int i = 0; i < 7; i++) {
        const int kt = (i < 6) ? kt_main + i : kt_side;
#pragma unroll
        for (int a = 0; a < 2; a++) {
          const f32x4 c0 = ca[i][2 * a], c1 = ca[i][2 * a + 1];
          const auto s0 = __builtin_amdgcn_permlane16_swap(pk(c0[0], c0[1]), pk(c1[0], c1[1]), false, false);
          const auto s1 = __builtin_amdgcn_permlane16_swap(pk(c0[2], c0[3]), pk(c1[2], c1[3]), false, false);
          st_sc1(v4u{relu2(s0[0]), relu2(s1[0]), relu2(s0[1]), relu2(s1[1])}, rws, lane * 16, WS_H + (ti * 4 + m * 2 + a) * HST + kt * 1024);
          SCHED0();
        }
      }
      if (HOLD) lds_signal(L, 16 + lw, tag);
      STAMP(3); STAMPC(2);
      own_flag = (GAS u64*)(ws + WS_HFLAG + ((ti * 2 + m) * 4 + (cg >> 1)) * 128 + ((cg & 1) * 2 + nh) * 8);
    } else {
      const int p = nh;
      const int ax0 = m * 8192 + (lane & 15) * 128 + ((q ^ ((lane & 15) >> 1)) * 16);
      int aw[2];
      { const int f = 2 * ((lane >> 3) & 1) + (q & 1), rb = 8 * q + ((lane & 15) >> 2);
        for (int i = 0; i < 2; i++) aw[i] = OFF_WM + rb * 384 + (lane & 3) * 8 + (((2 * p + i) ^ f) * 32); }
      const int ag = GATE_OFF + q * 16;
      v4u idn[2], sel[2];
#pragma unroll
      for (int v = 0; v < 2; v++)
#pragma unroll
        for (int jj = 0; jj < 4; jj++) {
          const int F = 8 * q + 2 * jj, c = 16 * v + (lane & 15);
          idn[v][jj] = ((c == F) ? 0x3f80u : 0u) | ((c == F + 1) ? 0x3f800000u : 0u);
        }
      asm volatile("" : "+v"(idn[0]), "+v"(idn[1]));
      f32x4 cq[2][4], ck[2][4], cv[2][4], cgate[2];
      for (int i = 0; i < 2; i++) { cgate[i] = f32x4{0, 0, 0, 0}; for (int t = 0; t < 4; t++) { cq[i][t] = cgate[i]; ck[i][t] = cgate[i]; cv[i][t] = cgate[i]; } }
      s16x8 xf[2][4], wf[2][6], gf[2];
      const int axp = ax0 + p * 4096, axn = ax0 + (1 - p) * 4096;
      auto rd = [&](int sb, int goff, int u, int bi) {
        LAS char* S = L + sb;
#pragma unroll
        for (int t = 0; t < 4; t++) xf[bi][t] = *(LAS s16x8*)(S + ((t < 2 ? axp : axn) ^ (u * 64)) + (t & 1) * 2048);
#pragma unroll
        for (int i = 0; i < 6; i++) wf[bi][i] = trfrag(S + aw[i & 1], (32 * u) * 384 + (i >> 1) * 128, (32 * u + 4) * 384 + (i >> 1) * 128);
        gf[bi] = *(LAS s16x8*)(L + ag + goff + u * 64);
      };
      auto mm = [&](int bi) {
#pragma unroll
        for (int i = 0; i < 2; i++)
#pragma unroll
          for (int t = 0; t < 4; t++) cq[i][t] = mfma16(wf[bi][i], xf[bi][t], cq[i][t]);
#pragma unroll
        for (int i = 0; i < 2; i++)
#pragma unroll
          for (int t = 0; t < 4; t++) ck[i][t] = mfma16(wf[bi][2 + i], xf[bi][t], ck[i][t]);
#pragma unroll
        for (int i = 0; i < 2; i++)
#pragma unroll
          for (int t = 0; t < 4; t++) cv[i][t] = mfma16(xf[bi][t], wf[bi][4 + i], cv[i][t]);
        cgate[0] = mfma16(gf[bi], xf[bi][0], cgate[0]);
        cgate[1] = mfma16(gf[bi], xf[bi][1], cgate[1]);
#pragma unroll
        for (int i = 0; i < 17; i++) { __builtin_amdgcn_sched_group_barrier(0x8, 1, 0); __builtin_amdgcn_sched_group_barrier(0x100, 1, 0); }
        __builtin_amdgcn_sched_group_barrier(0x8, 9, 0);
      };
      BAR();
      STAMP(1); STAMPC(0);
      int sb = slot_base(0);
      rd(sb, rota * 128, 0, 0);
      v4u wp[2];
      { const int wa = WPHI_OFF + 2 * p * 512 + (8 * hh + ((lane & 15) >> 2)) * 32 + (8 * ((lane >> 4) & 1) + 4 * ((lane & 3) >> 1)) * 2;
        const unsigned sg = ((lane >> 2) & 1) ? 0x80008000u : 0u;
#pragma unroll
        for (int s = 0; s < 2; s++) { wp[s] = __builtin_bit_cast(v4u, trfrag(L + wa, s * 512, s * 512 + 128)); wp[s] ^= sg; }
        asm volatile("" : "+v"(wp[0]), "+v"(wp[1])); }
      SCHED0();
#pragma unroll 1
      for (int s = 0; s < 8; s++) {
        rd(sb, ((s + rota) & 7) * 128, 1, 1); mm(0); SCHED0();
        if ((PFC | PFT) && s == 3) WAITV(0);
        BAR();
        sb = (sb == slot_base(0)) ? slot_base(1) : (sb == slot_base(1) ? slot_base(2) : slot_base(0));
        rd(sb, ((s + 1 + rota) & 7) * 128, 0, 0); mm(1); SCHED0();
      }
      STAMP(2); STAMPC(1);
      f32x4 kv[2][2], zz[2][2];
      {
        f32x16 P[4]; v4u ph[4][2], vb[4];
#pragma unroll
        for (int u = 0; u < 4; u++) P[u] = mfma32(wp[u & 1], to_frag32(ck[u & 1][u & 2], ck[u & 1][(u & 2) + 1]), zero16());
        SCHED0();
#pragma unroll
        for (int u = 0; u < 4; u++) vb[u] = pack44(cv[u & 1][u & 2], cv[u & 1][(u & 2) + 1]);
        if (lane < 16) { *(LAS float*)(L + SCR_GX + lw * 128 + lane * 4) = cgate[0][0]; *(LAS float*)(L + SCR_GX + lw * 128 + 64 + lane * 4) = cgate[1][0]; }
        SCHED0();
        STAMPC(3);
        softmax16x4(P, ph);
        STAMPC(4);
        const v4u ones = {0x3f803f80u, 0x3f803f80u, 0x3f803f80u, 0x3f803f80u};
        const f32x4 z4 = {0, 0, 0, 0};
        f32x4 Z[4][2][2];
#pragma unroll
        for (int u = 0; u < 4; u++) {
          v4u a0, a1;
#pragma unroll
          for (int i = 0; i < 4; i++) { const auto sw = __builtin_amdgcn_permlane16_swap(ph[u][0][i], ph[u][1][i], false, false); a0[i] = sw[0]; a1[i] = sw[1]; }
#pragma unroll
          for (int v = 0; v < 2; v++) { Z[u][0][v] = mfma16(a0, idn[v], z4); Z[u][1][v] = mfma16(a1, idn[v], z4); }
        }
#pragma unroll
        for (int u = 0; u < 4; u++)
#pragma unroll
          for (int v = 0; v < 2; v++) {
            const v4u a = pack44(Z[u][0][v], Z[u][1][v]);
            kv[u & 1][v] = mfma16(a, vb[u], u < 2 ? z4 : kv[u & 1][v]);
            zz[u & 1][v] = mfma16(a, ones, u < 2 ? z4 : zz[u & 1][v]);
          }
      }
      STAMPC(5);
      const int hd = 4 * cg + 2 * p + m;
      {
        LAS char* xw = L + SCR_XCH + lw * 2176;
        *(LAS f32x4*)(xw + lane * 16) = m ? kv[0][0] : kv[1][0];
        *(LAS f32x4*)(xw + 1024 + lane * 16) = m ? kv[0][1] : kv[1][1];
        if ((lane & 15) == 0) { *(LAS f32x4*)(xw + 2048 + q * 32) = m ? zz[0][0] : zz[1][0]; *(LAS f32x4*)(xw + 2048 + q * 32 + 16) = m ? zz[0][1] : zz[1][1]; }
        lds_signal(L, lw, tag);
        STAMPC(6);
        f32x4 k0 = m ? kv[1][0] : kv[0][0], k1 = m ? kv[1][1] : kv[0][1], y0 = m ? zz[1][0] : zz[0][0], y1 = m ? zz[1][1] : zz[0][1];
        lds_wait(L, lw ^ 1, tag);
        STAMPC(7);
        const LAS char* xr = L + SCR_XCH + (lw ^ 1) * 2176;
        k0 += *(const LAS f32x4*)(xr + lane * 16); k1 += *(const LAS f32x4*)(xr + 1024 + lane * 16);
        y0 += *(const LAS f32x4*)(xr + 2048 + q * 32); y1 += *(const LAS f32x4*)(xr + 2048 + q * 32 + 16);
        st_sc1(pack44(k0, k1), rws, lane * 16, WS_KVP + (hd * 16 + ti) * 1024);
        st_sc1(pack44(y0, y1), rws, (lane & 15) == 0 ? q * 16 : OOB, WS_ZP + (hd * 16 + ti) * 128);
      }
      SCHED0();
      STAMP(3); STAMPC(2);
      v4u pq[4][2];
      {
        f32x16 P[4];
#pragma unroll
        for (int u = 0; u < 4; u++) P[u] = mfma32(wp[u & 1], to_frag32(cq[u & 1][u & 2], cq[u & 1][(u & 2) + 1]), zero16());
#pragma unroll
        for (int u = 0; u < 2; u++)
#pragma unroll
          for (int jj = 0; jj < 4; jj++) {
            const int f0 = 16 * (jj >> 1) + 4 * q + 2 * (jj & 1), rho = lane & 15;
            sel[u][jj] = ((f0 == 16 * u + rho) ? 0x3f80u : 0u) | ((f0 + 1 == 16 * u + rho) ? 0x3f800000u : 0u);
          }
        asm volatile("" : "+v"(sel[0]), "+v"(sel[1]));
        asm volatile("" : "+v"(P[0]), "+v"(P[1]), "+v"(P[2]), "+v"(P[3]));
        WAITV(0);
        if (lane == 0) __hip_atomic_store((GAS u64*)(ws + WS_KFLAG + hd * 128 + ti * 8), tag, RLX_AGENT);
        STAMP(4);
        asm volatile("" : "+v"(P[0]), "+v"(P[1]), "+v"(P[2]), "+v"(P[3]));
        softmax16x4(P, pq);
#pragma unroll
        for (int u = 0; u < 4; u++) asm volatile("" : "+v"(pq[u][0]), "+v"(pq[u][1]));
      }
      lds_wait(L, lw ^ 2, tag);
      float gt[2];
      { const float l0 = *(const LAS float*)(L + SCR_GX + lw * 128 + (lane & 31) * 4), l1 = *(const LAS float*)(L + SCR_GX + (lw ^ 2) * 128 + (lane & 31) * 4);
        gt[0] = __builtin_amdgcn_rcpf(1.f + __builtin_amdgcn_exp2f(-1.4426950408889634f * l0));
        gt[1] = __builtin_amdgcn_rcpf(1.f + __builtin_amdgcn_exp2f(-1.4426950408889634f * l1));
        asm volatile("" : "+v"(gt[0]), "+v"(gt[1])); }
      STAMP(5);
      {
        const int hg = 4 * cg + lw, rho = lane & 15;
        poll_flags((const GAS u64*)(ws + WS_KFLAG + hg * 128 + (lane & 15) * 8), tag);
        STAMP(6);
        const v4u zp = ld_sc1(rws, (lane & 15) * 128 + q * 16, WS_ZP + hg * 2048);
        v4u kp[16];
#pragma unroll
        for (int i = 0; i < 16; i++) kp[i] = ld_sc1(rws, lane * 16, WS_KVP + (hg * 16 + ((i + ti) & 15)) * 1024);
        SCHED0();
        LAS char* ks = L + SCR_KS + lw * 1088;
        float z[8];
#pragma unroll
        for (int jj = 0; jj < 4; jj++) { z[2 * jj] = row_sum16(__uint_as_float(zp[jj] << 16)); z[2 * jj + 1] = row_sum16(__uint_as_float(zp[jj] & 0xffff0000u)); }
        if (rho == 0) {
          LAS char* zr = ks + 1024 + q * 8;
          *(LAS v2u*)(zr) = v2u{pk(z[0], z[1]), pk(z[2], z[3])};
          *(LAS v2u*)(zr + 32) = v2u{pk(z[4], z[5]), pk(z[6], z[7])};
        }
        SCHED0();
        f32x4 t0 = {0, 0, 0, 0}, t1 = {0, 0, 0, 0};
#pragma unroll
        for (int i = 0; i < 16; i++) { t0 = mfma16(sel[0], kp[i], t0); t1 = mfma16(sel[1], kp[i], t1); }
        *(LAS v2u*)(ks + rho * 64 + q * 8) = v2u{pk(t0[0], t0[1]), pk(t0[2], t0[3])};
        *(LAS v2u*)(ks + rho * 64 + 32 + q * 8) = v2u{pk(t1[0], t1[1]), pk(t1[2], t1[3])};
        lds_signal(L, 4 + lw, tag);
      }
      STAMP(7);
      { const int rho = lane & 31, krow = rho < 16 ? (8 * ((rho >> 2) & 1) + (rho & 3) + 4 * (rho >> 3)) : 16;
        lds_wait(L, 4 + (lw ^ 1), tag);
        const LAS char* ks = L + SCR_KS + 2 * p * 1088 + krow * 64 + hh * 32;
        f32x16 o[4];
#pragma unroll
        for (int s = 0; s < 2; s++) {
          const v4u k0 = *(const LAS v4u*)(ks + s * 1088), k1 = *(const LAS v4u*)(ks + s * 1088 + 16);
#pragma unroll
          for (int t = 0; t < 2; t++) { o[2 * t + s] = mfma32(k0, pq[2 * t + s][0], zero16()); o[2 * t + s] = mfma32(k1, pq[2 * t + s][1], o[2 * t + s]); }
        }
#pragma unroll
        for (int u = 0; u < 4; u++) {
          const float sc = gt[u >> 1] * __builtin_amdgcn_rcpf(o[u][8] + 1e-6f);
#pragma unroll
          for (int i = 0; i < 8; i++) o[u][i] *= sc;
          st_sc1(pack8(o[u], 0), rws, lane * 16, WS_O + (ti * 4 + m * 2 + ((u >> 1) ^ p)) * OST + (4 * cg + 2 * p + (u & 1)) * 1024);
        }
        const unsigned go = ((cg & 1) == 0 && p == 0) ? (WS_G + ((ti * 2 + m) * 4 + (cg >> 1)) * 256 + lane * 4) : OOB;
        __builtin_amdgcn_raw_buffer_store_b32(__float_as_uint(1.f - (hh ? gt[1] : gt[0])), rws, go, 0, 16);
        own_flag = (GAS u64*)(ws + WS_OFLAG + ((ti * 2 + m) * 4 + (cg >> 1)) * 128 + ((cg & 1) * 2 + p) * 8);
      }
    }
    publish_then_poll(own_flag, (const GAS u64*)(ws + hflag_off), tag, lane);
    STAMP(8);
    v4u av[10][2];
#pragma unroll
    for (int j = 0; j < 10; j++) { const unsigned so = hbase + ktf(18 + j) * 1024; av[j][0] = ld_sc1(rws, lane * 16, so); av[j][1] = ld_sc1(rws, lane * 16, so + HST); }
    SCHED0();
    lds_wait(L, 8 + lw, tag);
    acc[0][0] = zero16(); acc[0][1] = zero16(); acc[1][0] = zero16(); acc[1][1] = zero16();
    auto zoff = [&](int j) { return j < 3 ? zb1 + j * 2048 : (j < 9 ? zb2 + (j - 3) * 2048 : (j < 14 ? zb3 + (j - 9) * 2048 : zb4 + (j - 14) * 2048)); };
#pragma unroll
    for (int j = 0; j < 10; j++) {
      const s16x8 w0 = trfrag(L + zoff(j) + awB0, 0, 512), w1f = trfrag(L + zoff(j) + awB1, 0, 512);
      acc[0][0] = mfma32(w0, av[j][0], acc[0][0]);
      acc[1][0] = mfma32(w1f, av[j][0], acc[1][0]);
      acc[0][1] = mfma32(w0, av[j][1], acc[0][1]);
      acc[1][1] = mfma32(w1f, av[j][1], acc[1][1]);
    }
    SCHED0();
    lds_signal(L, 12 + lw, tag);
    STAMP(9);
    s16x8 wa[8][2];
#pragma unroll
    for (int j = 0; j < 8; j++) { wa[j][0] = trfrag(L + zoff(10 + j) + awB0, 0, 512); wa[j][1] = trfrag(L + zoff(10 + j) + awB1, 0, 512); }
    SCHED0();
    poll_flags((const GAS u64*)(ws + hflag_off + (WS_OFLAG - WS_HFLAG)), tag);
    STAMP(10);
    v4u ov[8][2];
    const unsigned go = WS_G + (t64 * 4 + lw) * 256;
    const float g0 = __uint_as_float(__builtin_amdgcn_raw_buffer_load_b32(rws, (lane & 31) * 4, go, 16));
    const float g1 = __uint_as_float(__builtin_amdgcn_raw_buffer_load_b32(rws, (lane & 31) * 4, go + 128, 16));
    const unsigned obase = WS_O + t64 * 2 * OST + 8 * lw * 1024;
#pragma unroll
    for (int j = 0; j < 8; j++) { const unsigned so = obase + ((j + rotc) & 7) * 1024; ov[j][0] = ld_sc1(rws, lane * 16, so); ov[j][1] = ld_sc1(rws, lane * 16, so + OST); }
    SCHED0();
    if (lw == 0) *(LAS float*)(L + SCR_G2 + lane * 4) = hh ? g1 : g0;
#pragma unroll
    for (int i = 0; i < 16; i++) { acc[0][0][i] *= g0; acc[1][0][i] *= g0; acc[0][1][i] *= g1; acc[1][1][i] *= g1; }
    SCHED0();
#pragma unroll
    for (int j = 0; j < 8; j++) {
      acc[0][0] = mfma32(wa[j][0], ov[j][0], acc[0][0]);
      acc[1][0] = mfma32(wa[j][1], ov[j][0], acc[1][0]);
      acc[0][1] = mfma32(wa[j][0], ov[j][1], acc[0][1]);
      acc[1][1] = mfma32(wa[j][1], ov[j][1], acc[1][1]);
    }
  }
  STAMP(11);
  {
    const int half = wid >> 2;
    const int pa = half ? zb2 : zb3, pb = half ? zb2 + 8192 : zb4, pc = half ? zb1 : zb4 + 4096;
    LAS char* wl = L + lane * 16;
#pragma unroll
    for (int d = 0; d < 4; d++) {
      const f32x16& t = acc[d >> 1][d & 1];
      LAS char* w = wl + (d < 2 ? pa + d * 4096 : (d == 2 ? pb : pc));
#pragma unroll
      for (int i = 0; i < 4; i++) *(LAS f32x4*)(w + i * 1024) = f32x4{t[4 * i], t[4 * i + 1], t[4 * i + 2], t[4 * i + 3]};
    }
    BAR();
    STAMP(12);
    const float g = *(const LAS float*)(L + SCR_G2 + (lw & 1) * 128 + (lane & 31) * 4);
    const LAS char* rc = L + lane * 16 + half * 2048 + (lw < 2 ? lw * 4096 : 20 * 2048 + (lw - 2) * 4096);
    const LAS char* rl = L + lane * 16 + half * 2048 + (lw < 3 ? 56 * 2048 + lw * 4096 : 44 * 2048);
    const int sc = lw < 2 ? 5 * 2048 : 4 * 2048, sl = lw < 3 ? 6 * 2048 : 3 * 2048;
    f32x4 c0 = *(const LAS f32x4*)rc, c1 = *(const LAS f32x4*)(rc + 1024), l0 = *(const LAS f32x4*)rl, l1 = *(const LAS f32x4*)(rl + 1024);
#pragma unroll
    for (int i = 1; i < 4; i++) {
      c0 += *(const LAS f32x4*)(rc + i * sc); c1 += *(const LAS f32x4*)(rc + i * sc + 1024);
      l0 += *(const LAS f32x4*)(rl + i * sl); l1 += *(const LAS f32x4*)(rl + i * sl + 1024);
    }
    c0 += l0 * g; c1 += l1 * g;
    const unsigned a0 = pk(c0[0], c0[1]), a1 = pk(c0[2], c0[3]), b0 = pk(c1[0], c1[1]), b1 = pk(c1[2], c1[3]);
    const auto r0 = __builtin_amdgcn_permlane32_swap(a0, b0, false, false);
    const auto r1 = __builtin_amdgcn_permlane32_swap(a1, b1, false, false);
    const int tok = t64 * 64 + (lw & 1) * 32 + (lane & 31);
    const int col = jc * 64 + (lw >> 1) * 32 + half * 16 + hh * 8;
    const rsrc_t ro = __builtin_amdgcn_make_buffer_rsrc((void*)out, (short)0, 2048 * 512 * 2, 0x00020000);
    __builtin_amdgcn_raw_buffer_store_b128(v4u{r0[0], r1[0], r0[1], r1[1]}, ro, (tok * 512 + col) * 2, 0, OUTSC);
  }
  STAMP(13);
  STAMP_FLUSH;
}

#define DIAG_LAUNCH

extern "C" void kernel_launch(void* const* d_in, const int* in_sizes, int n_in, void* d_out, int out_size, void* d_ws, size_t ws_size,
                              hipStream_t stream) {
  (void)hipFuncSetAttribute((const void*)fused_kernel, hipFuncAttributeMaxDynamicSharedMemorySize, LDS_BYTES);
  fused_kernel<<<256, 512, LDS_BYTES, stream>>>((const u16*)d_in[0], (const u16*)d_in[1], (const u16*)d_in[2], (const u16*)d_in[3],
                                                (const u16*)d_in[5], (const u16*)d_in[6], (const u16*)d_in[7], (u16*)d_out, (char*)d_ws);
  DIAG_LAUNCH
}
```

```cpp
#include <hip/hip_runtime.h>
#include <hip/hip_bf16.h>

typedef unsigned short u16;
typedef unsigned long long u64;
typedef unsigned v4u __attribute__((ext_vector_type(4)));
typedef unsigned v2u __attribute__((ext_vector_type(2)));
typedef short s16x2 __attribute__((ext_vector_type(2)));
typedef short s16x4 __attribute__((ext_vector_type(4)));
typedef short s16x8 __attribute__((ext_vector_type(8)));
typedef __bf16 bf16x8 __attribute__((ext_vector_type(8)));
typedef __bf16 bf16x2 __attribute__((ext_vector_type(2)));
typedef float f32x2 __attribute__((ext_vector_type(2)));
typedef float f32x4 __attribute__((ext_vector_type(4)));
typedef float f32x16 __attribute__((ext_vector_type(16)));
typedef __amdgpu_buffer_rsrc_t rsrc_t;
#define LAS __attribute__((address_space(3)))
#define GAS __attribute__((address_space(1)))
#define DI __device__ __forceinline__
#define RLX_AGENT __ATOMIC_RELAXED, __HIP_MEMORY_SCOPE_AGENT

#define DIAG 0
#define PFW 1
#define HOLD 1
#define PFC 1
#define PFT 1
#define PFM 1
#define ROTB 1
#define OUTSC 0
#define EXP_ROLE 0
#ifndef EXIT_A
#define EXIT_A 0
#endif
#define LSLEEP 0
#define CPRIO 0
#define SYNCB 0
#define EXP_MASK 0
#define NOZONE 0
#define CODE_KB 31

constexpr unsigned WS_EPOCH = 0;
constexpr unsigned WS_HFLAG = 4096;
constexpr unsigned WS_OFLAG = WS_HFLAG + 128 * 128;
constexpr unsigned WS_KFLAG = WS_OFLAG + 128 * 128;
constexpr unsigned WS_G     = WS_KFLAG + 32 * 128;
constexpr unsigned WS_ZP    = WS_G + 128 * 256;
constexpr unsigned WS_KVP   = WS_ZP + 512 * 128;
constexpr unsigned WS_O     = WS_KVP + 512 * 1024;
constexpr unsigned OST = 33 * 1024, HST = 113 * 1024;
constexpr unsigned WS_H     = WS_O + 64 * OST;
constexpr unsigned WS_END   = WS_H + 64 * HST;
constexpr unsigned WS_DIAG  = WS_END;
constexpr unsigned OOB = 0x7ffffff0u;

constexpr int BLK = 2048;
constexpr int OFF_WM = 16384, OFF_WS = 40960;
constexpr int LDS_BYTES = 163840;
__host__ __device__ constexpr int slot_base(int i) { return i == 0 ? 36 * BLK : (i == 1 ? 58 * BLK : 14 * BLK); }
constexpr int GATE_OFF = 0;
constexpr int SYNC = 1024;
constexpr int SY_LANDED = 0, SY_DONE = 4, SY_XCH = 8, SY_KS = 12, SY_WA = 16, SY_WB = 20, SY_CF = 24, SY_ST = 28;
constexpr int PF_OFF = 1280;
constexpr int WPHI_OFF = slot_base(0) + OFF_WS;
constexpr int SCR = 73 * BLK;
constexpr int SCR_XCH = SCR;
constexpr int SCR_KS = SCR + 8704;
constexpr int SCR_GX = SCR_KS + 4352;
constexpr int SCR_G2 = SCR_GX + 512;
constexpr unsigned SPIN_CAP = 1u << 16;

DI void dma(unsigned lds, int imm, unsigned voff, rsrc_t r, unsigned soff) {
  asm volatile("s_add_i32 m0, %0, %4\n\ts_nop 0\n\tbuffer_load_dwordx4 %1, %2, %3 offen lds" ::"s"(lds), "v"(voff), "s"(r), "s"(soff), "i"(imm) : "memory", "scc");
}
DI void touch(unsigned lds, unsigned voff, rsrc_t r) {
  asm volatile("s_mov_b32 m0, %0\n\ts_nop 0\n\tbuffer_load_dword %1, %2, 0 offen lds" ::"s"(lds), "v"(voff), "s"(r) : "memory");
}
#define OPAQUE_S(x) asm volatile("" : "+s"(x))
DI s16x8 trfrag(LAS char* p, int off0, int off1) {
  s16x4 a = __builtin_amdgcn_ds_read_tr16_b64_v4i16((LAS s16x4*)(p + off0));
  s16x4 b = __builtin_amdgcn_ds_read_tr16_b64_v4i16((LAS s16x4*)(p + off1));
  return __builtin_shufflevector(a, b, 0, 1, 2, 3, 4, 5, 6, 7);
}
template <class A, class B> DI f32x4 mfma16(A a, B b, f32x4 c) {
  return __builtin_amdgcn_mfma_f32_16x16x32_bf16(__builtin_bit_cast(bf16x8, a), __builtin_bit_cast(bf16x8, b), c, 0, 0, 0);
}
template <class A, class B> DI f32x16 mfma32(A a, B b, f32x16 c) {
  return __builtin_amdgcn_mfma_f32_32x32x16_bf16(__builtin_bit_cast(bf16x8, a), __builtin_bit_cast(bf16x8, b), c, 0, 0, 0);
}
DI unsigned pk(float a, float b) { f32x2 v = {a, b}; return __builtin_bit_cast(unsigned, __builtin_convertvector(v, bf16x2)); }
DI unsigned relu2(unsigned v) { return __builtin_bit_cast(unsigned, __builtin_elementwise_max(__builtin_bit_cast(s16x2, v), s16x2{0, 0})); }
DI v4u pack44(const f32x4& a, const f32x4& b) { return v4u{pk(a[0], a[1]), pk(a[2], a[3]), pk(b[0], b[1]), pk(b[2], b[3])}; }
DI v4u to_frag32(const f32x4& c0, const f32x4& c1) {
  const auto s0 = __builtin_amdgcn_permlane16_swap(pk(c0[0], c0[1]), pk(c1[0], c1[1]), false, false);
  const auto s1 = __builtin_amdgcn_permlane16_swap(pk(c0[2], c0[3]), pk(c1[2], c1[3]), false, false);
  return v4u{s0[0], s1[0], s0[1], s1[1]};
}
DI v4u pack8(const f32x16& x, int s) {
  return v4u{pk(x[8 * s], x[8 * s + 1]), pk(x[8 * s + 2], x[8 * s + 3]), pk(x[8 * s + 4], x[8 * s + 5]), pk(x[8 * s + 6], x[8 * s + 7])};
}
DI f32x16 zero16() { f32x16 z; for (int i = 0; i < 16; i++) z[i] = 0.f; return z; }
#define WAITV(n) asm volatile("s_waitcnt vmcnt(" #n ")" ::: "memory")
#define WAITL() asm volatile("s_waitcnt lgkmcnt(0)" ::: "memory")
#define BAR() do { asm volatile("s_waitcnt lgkmcnt(0)" ::: "memory"); __builtin_amdgcn_s_barrier(); asm volatile("" ::: "memory"); } while (0)
#define SCHED0() __builtin_amdgcn_sched_barrier(0)
#define SGB(mask, n) __builtin_amdgcn_sched_group_barrier(mask, n, 0)
DI v4u ld_sc1(rsrc_t r, unsigned voff, unsigned soff) { return __builtin_amdgcn_raw_buffer_load_b128(r, voff, soff, 16); }
DI void st_sc1(v4u v, rsrc_t r, unsigned voff, unsigned soff) {
  __builtin_amdgcn_raw_buffer_store_b128(v, r, voff, soff, 16);
  asm volatile("s_nop 1" ::"v"(v) : "memory");
}

DI void poll_flags(const GAS u64* p, u64 tag) {
#pragma unroll 1
  for (unsigned spins = 0; spins < SPIN_CAP; ++spins) {
    u64 v = __hip_atomic_load(p, RLX_AGENT);
    if (__all(v == tag)) break;
    __builtin_amdgcn_s_sleep(1);
  }
  __builtin_amdgcn_fence(__ATOMIC_ACQUIRE, "wavefront");
  asm volatile("" ::: "memory");
}
DI void publish_then_poll(GAS u64* own, const GAS u64* p, u64 tag, int lane) {
  u64 v = __hip_atomic_load(p, RLX_AGENT);
  asm volatile("s_waitcnt vmcnt(0)" : "+v"(v) :: "memory");
  if (lane == 0) __hip_atomic_store(own, tag, RLX_AGENT);
  if (!__all(v == tag)) poll_flags(p, tag);
  __builtin_amdgcn_fence(__ATOMIC_ACQUIRE, "wavefront");
  asm volatile("" ::: "memory");
}
DI u64 load_epoch(const char* ws) {
  u64 e;
  asm volatile("s_load_dwordx2 %0, %1, 0x0\n\ts_waitcnt lgkmcnt(0)" : "=s"(e) : "s"(ws) : "memory");
  return e + 1;
}
DI float row_sum16(float x) {
  x += __uint_as_float(__builtin_amdgcn_mov_dpp(__float_as_uint(x), 0x128, 0xf, 0xf, true));
  x += __uint_as_float(__builtin_amdgcn_mov_dpp(__float_as_uint(x), 0x124, 0xf, 0xf, true));
  x += __uint_as_float(__builtin_amdgcn_mov_dpp(__float_as_uint(x), 0x122, 0xf, 0xf, true));
  x += __uint_as_float(__builtin_amdgcn_mov_dpp(__float_as_uint(x), 0x121, 0xf, 0xf, true));
  return x;
}
DI unsigned sy_read(LAS char* L, int w) { return *(volatile LAS unsigned*)(L + SYNC + w * 4); }
DI void sy_wait(LAS char* L, int w, unsigned need) {
#pragma unroll 1
  for (unsigned spins = 0; spins < (SPIN_CAP << 6); ++spins)
    if (__builtin_amdgcn_readfirstlane(sy_read(L, w)) >= need) break;
  asm volatile("" ::: "memory");
}
DI bool all4(unsigned v, unsigned need) { return (((v | 0x80808080u) - need * 0x01010101u) & 0x80808080u) == 0x80808080u; }
DI void sy_wait4(LAS char* L, int w, unsigned need) {
#pragma unroll 1
  for (unsigned spins = 0; spins < (SPIN_CAP << 6); ++spins)
    if (all4(__builtin_amdgcn_readfirstlane(sy_read(L, w)), need)) break;
  asm volatile("" ::: "memory");
}
DI void sy_wait4s(LAS char* L, int w, unsigned need) {
#pragma unroll 1
  for (unsigned spins = 0; spins < (SPIN_CAP << 6); ++spins) {
    if (all4(__builtin_amdgcn_readfirstlane(sy_read(L, w)), need)) break;
    if (LSLEEP) __builtin_amdgcn_s_sleep(LSLEEP);
  }
  asm volatile("" ::: "memory");
}
DI void sy_setb(LAS char* L, int w, int byte, unsigned v) {
  asm volatile("s_waitcnt lgkmcnt(0)" ::: "memory");
  if ((threadIdx.x & 63) == 0) *(volatile LAS unsigned char*)(L + SYNC + w * 4 + byte) = (unsigned char)v;
}
DI void sy_set(LAS char* L, int w, unsigned v) {
  asm volatile("s_waitcnt lgkmcnt(0)" ::: "memory");
  if ((threadIdx.x & 63) == 0) *(volatile LAS unsigned*)(L + SYNC + w * 4) = v;
}
#define STAMP(i) do {} while (0)
#define STAMPC(i) do {} while (0)
#define STAMP_DECL
#define STAMP_FLUSH

template <int N> DI void softmax16(f32x16* p, v4u (*o)[2]) {
  const float L2E = 1.4426950408889634f;
  float m[N]; f32x2 e[N][8], s[N];
#pragma unroll
  for (int u = 0; u < N; u++) m[u] = fmaxf(fmaxf(p[u][0], p[u][1]), p[u][2]);
#pragma unroll
  for (int i = 3; i < 15; i += 2)
#pragma unroll
    for (int u = 0; u < N; u++) m[u] = fmaxf(fmaxf(m[u], p[u][i]), p[u][i + 1]);
#pragma unroll
  for (int u = 0; u < N; u++) m[u] = -L2E * fmaxf(m[u], p[u][15]);
#pragma unroll
  for (int i = 0; i < 8; i++)
#pragma unroll
    for (int u = 0; u < N; u++) {
      const f32x2 t = __builtin_elementwise_fma(f32x2{p[u][2 * i], p[u][2 * i + 1]}, f32x2{L2E, L2E}, f32x2{m[u], m[u]});
      e[u][i] = f32x2{__builtin_amdgcn_exp2f(t[0]), __builtin_amdgcn_exp2f(t[1])};
      s[u] = i ? s[u] + e[u][i] : e[u][i];
    }
#pragma unroll
  for (int u = 0; u < N; u++) {
    const float r = __builtin_amdgcn_rcpf(s[u][0] + s[u][1]);
#pragma unroll
    for (int i = 0; i < 8; i++) { const f32x2 t = e[u][i] * r; o[u][i >> 2][i & 3] = pk(t[0], t[1]); }
  }
}

__global__ __launch_bounds__(512, 2) void fused_kernel(const u16* __restrict__ x, const u16* __restrict__ wqkv, const u16* __restrict__ wphi,
                                                       const u16* __restrict__ wo, const u16* __restrict__ w1, const u16* __restrict__ w2,
                                                       const u16* __restrict__ gw, u16* __restrict__ out, char* __restrict__ ws) {
  extern __shared__ __attribute__((aligned(16))) char shm[];
  LAS char* L = (LAS char*)shm;
  const unsigned ldsbase = (unsigned)(size_t)L;
  if (EXP_MASK && (blockIdx.x & EXP_MASK)) return;
  u64 pc0 = 0;
  if (PFC) asm volatile("s_getpc_b64 %0" : "=s"(pc0));
  STAMP_DECL
  STAMP(0);
  const int tid = threadIdx.x, lane = tid & 63;
  const int wid = __builtin_amdgcn_readfirstlane(tid >> 6);
  const int lw = wid & 3;
  const int b = blockIdx.x, xcd = b & 7, r = b >> 3;
  const bool ffn = EXP_ROLE == 1 ? true : (EXP_ROLE == 2 ? false : (r >> 4) == 0);
  const int ti = (xcd & 1) * 8 + (r & 7), cg = (xcd >> 1) * 2 + ((r >> 3) & 1);
#define RSRC(p, bytes) __builtin_amdgcn_make_buffer_rsrc((void*)(p), (short)0, (bytes), 0x00020000)
#define RX RSRC(x, 2048 * 512 * 2)
#define RW1 RSRC(w1, 512 * 1792 * 2)
#define RWQ RSRC(wqkv, 512 * 1536 * 2)
#define RW2 RSRC(w2, 1792 * 512 * 2)
#define RWO RSRC(wo, 512 * 512 * 2)

  const rsrc_t rx = RX;
  const rsrc_t rw = RSRC(ffn ? w1 : wqkv, ffn ? 512 * 1792 * 2 : 512 * 1536 * 2);
  const int RS = ffn ? 3584 : 3072, LSTR = ffn ? 128 : 1024;
  const unsigned lb1 = ldsbase + lw * 1024, lb2 = ldsbase + lw * 2048, lb4 = ldsbase + lw * 4096;
  unsigned sx = ti * 131072 + lw * 32768, sw = 16 * lw * RS + (ffn ? (cg * 448 + (cg & 1) * 64) : cg * 128), ss = 16 * lw * 3584 + cg * 448 + ((cg & 1) ? 0 : 384);
  unsigned vx[2], vw[2], vs;
  auto issue_x = [&](int s) {
    OPAQUE_S(sx);
#pragma unroll
    for (int i = 0; i < 4; i++) dma(lb4, slot_base(s % 3) + i * 1024, vx[i & 1], rx, sx + i * 8192 + s * 128);
  };
  auto issue_w = [&](int s) {
    const int sl = slot_base(s % 3);
    OPAQUE_S(ss); OPAQUE_S(sw);
    const unsigned so = sw + s * 64 * RS;
#pragma unroll
    for (int l = 0; l < 3; l++) { dma(lb2, sl + OFF_WM + l * 8192, vw[0], rw, so + l * LSTR); dma(lb2, sl + OFF_WM + l * 8192 + 1024, vw[1], rw, so + l * LSTR); }
    if (ffn) dma(lb1, sl + OFF_WS, vs, rw, ss + s * 64 * 3584);
  };
  if (wid >= 4) {
    for (int p = 0; p < 2; p++) {
      vx[p] = (lane >> 3) * 1024 + (((lane & 7) ^ (4 * p + (lane >> 4))) * 16);
      vw[p] = (8 * p + (lane >> 3)) * RS + (((lane & 7) ^ ((2 * ((lane >> 4) & 1) + p) << 1)) * 16);
    }
    vs = (lane >> 2) * 3584 + (((lane & 3) ^ ((lane >> 5) << 1)) * 16);
    issue_x(0); issue_w(0);
    if (!ffn && lw < 3) dma(lb1 + (lw ? WPHI_OFF - 1024 : GATE_OFF), 0, lane * 16, RSRC(lw ? wphi : gw, lw ? 32 * 256 * 2 : 1024), lw ? cg * 2048 + (lw - 1) * 1024 : 0);
    issue_x(1); issue_w(1);
  }
  SCHED0();

  const int t64 = xcd * 4 + (r & 3), jc = r >> 2;
  int rotb = ROTB ? (7 * (r & 3) + ((7 * (r >> 2)) >> 1)) % 28 : 0;
  const int rotc = ROTB ? (2 * (r & 3) + (r >> 2)) & 7 : 0;
  const rsrc_t rws = RSRC(ws, WS_END);
  u64 tag;
  f32x16 acc[2][2];
  const int hh = lane >> 5;
  const int awB0 = (8 * hh + ((lane & 15) >> 2)) * 128 + ((((lane >> 4) & 1) ^ (2 * ((lane >> 3) & 1) + hh)) * 32) + (lane & 3) * 8;
  const int awB1 = awB0 ^ 64;
  const int zb1 = (1 + 3 * lw) * BLK, zb2 = (13 + 5 * lw) * BLK, zb3 = (33 + 6 * lw) * BLK, zb4 = (57 + 4 * lw) * BLK;
  auto zbase = [&](int j) { return j < 3 ? zb1 : (j < 8 ? zb2 : (j < 14 ? zb3 : zb4)); };
  auto zoffs = [&](int j) { return (j < 3 ? j : (j < 8 ? j - 3 : (j < 14 ? j - 8 : j - 14))) * BLK; };
  const unsigned hflag_off = WS_HFLAG + (t64 * 4 + lw) * 128 + (lane & 3) * 8;
  const unsigned hbase = WS_H + t64 * 2 * HST + 28 * lw * 1024;
  auto ktf = [&](int j) { if (ROTB) OPAQUE_S(rotb); const int k = j + rotb; return k >= 28 ? k - 28 : k; };
  const unsigned pl = ldsbase + PF_OFF;

  if (wid >= 4) {
    const rsrc_t rw2 = RW2, rwo = RWO;
    unsigned vb[2];
    for (int e = 0; e < 2; e++) vb[e] = (8 * e + (lane >> 3)) * 1024 + (((lane & 7) ^ ((2 * ((lane >> 4) & 1) + e) << 1)) * 16);
    const unsigned s2 = 28 * lw * 16384 + jc * 128, so8 = 8 * lw * 16384 + jc * 128;
    auto wblk_own = [&](int j) {
      if (NOZONE) return;
      const unsigned so = s2 + ktf(j) * 16384;
      dma(ldsbase + zbase(j), zoffs(j), vb[0], rw2, so); dma(ldsbase + zbase(j), zoffs(j) + 1024, vb[1], rw2, so);
    };
    auto wblk_c = [&](int j) {
      if (j < 10) { const unsigned so = s2 + ktf(18 + j) * 16384; dma(ldsbase + zbase(j), zoffs(j), vb[0], rw2, so); dma(ldsbase + zbase(j), zoffs(j) + 1024, vb[1], rw2, so); }
      else { const unsigned so = so8 + ((j - 10 + rotc) & 7) * 16384; dma(ldsbase + zbase(j), zoffs(j), vb[0], rwo, so); dma(ldsbase + zbase(j), zoffs(j) + 1024, vb[1], rwo, so); }
    };
    s16x8 wr[18][2];
    auto wread = [&](int j) { wr[j][0] = trfrag(L + zbase(j) + awB0, zoffs(j), zoffs(j) + 512); wr[j][1] = trfrag(L + zbase(j) + awB1, zoffs(j), zoffs(j) + 512); };
    auto tw = [&](int i) {
      if (!PFW) return;
      if (i == 0) touch(pl, lane < 52 ? (r * 448 + lw * 52 + lane) * 128 : OOB, rw2);
      else if (i < 4) touch(pl, lane < 20 ? (r * 448 + 128 + i * 80 + lw * 20 + lane) * 128 : OOB, rw2);
      else touch(pl, lane < 16 ? (r * 128 + (i - 4) * 64 + lw * 16 + lane) * 128 : OOB, rwo);
    };
    tw(0);
    tag = load_epoch(ws);
#define WAITS(a, b) do { if (ffn) WAITV(b); else WAITV(a); } while (0)
    if (PFW) WAITS(11, 12); else WAITS(10, 11);
    BAR();
    STAMP(1);
    issue_x(2); issue_w(2); tw(1);
#pragma unroll
    for (int s = 0; s < 5; s++) {
      if (PFW) WAITS(12, 13); else WAITS(10, 11);
      BAR();
      issue_x(s + 3); issue_w(s + 3);
      if (s < 4) tw(s + 2);
    }
    wblk_own(0);
    if (PFW) WAITS(13, 14); else WAITS(12, 13);
    BAR();
#pragma unroll
    for (int j = 1; j < 8; j++) wblk_own(j);
    WAITV(16);
    BAR();
#pragma unroll
    for (int j = 8; j < 14; j++) wblk_own(j);
    BAR();
    STAMP(2); STAMPC(0);
    if (EXIT_A) { WAITV(0); STAMP(13); STAMP_FLUSH; return; }
#pragma unroll
    for (int j = 14; j < 18; j++) wblk_own(j);
    WAITV(20);
    STAMPC(1);
#pragma unroll
    for (int j = 0; j < 8; j++) wread(j);
    SCHED0();
    WAITV(8);
#pragma unroll
    for (int j = 8; j < 14; j++) wread(j);
    SCHED0();
    WAITV(0);
#pragma unroll
    for (int j = 14; j < 18; j++) wread(j);
    WAITL(); SCHED0();
    STAMPC(2);
    if (HOLD && ffn) sy_wait(L, SY_ST + lw, 1);
    STAMPC(3);
#pragma unroll
    for (int j = 0; j < 18; j++) wblk_c(j);
    STAMP(3); STAMPC(4);
    {
      u64 v = __hip_atomic_load((const GAS u64*)(ws + hflag_off), RLX_AGENT);
      WAITV(17);
      sy_set(L, SY_WA + lw, 1);
      STAMPC(5);
      asm volatile("s_waitcnt vmcnt(0)" : "+v"(v) :: "memory");
      sy_set(L, SY_WB + lw, 1);
      STAMP(5); STAMPC(6);
      if (!__all(v == tag)) poll_flags((const GAS u64*)(ws + hflag_off), tag);
      __builtin_amdgcn_fence(__ATOMIC_ACQUIRE, "wavefront");
      asm volatile("" ::: "memory");
    }
    STAMP(6);
    v4u av[18][2];
    constexpr int W0 = 4;
#pragma unroll
    for (int j = 0; j < W0; j++) { const unsigned so = hbase + ktf(j) * 1024; av[j][0] = ld_sc1(rws, lane * 16, so); av[j][1] = ld_sc1(rws, lane * 16, so + HST); }
    SCHED0();
    acc[0][0] = zero16(); acc[0][1] = zero16(); acc[1][0] = zero16(); acc[1][1] = zero16();
#pragma unroll
    for (int j = 0; j < 18; j++) {
      acc[0][0] = mfma32(wr[j][0], av[j][0], acc[0][0]);
      acc[1][0] = mfma32(wr[j][1], av[j][0], acc[1][0]);
      acc[0][1] = mfma32(wr[j][0], av[j][1], acc[0][1]);
      acc[1][1] = mfma32(wr[j][1], av[j][1], acc[1][1]);
      SCHED0();
#pragma unroll
      for (int n = W0 + 2 * j; n < W0 + 2 * j + 2; n++)
        if (n < 18) { const unsigned so = hbase + ktf(n) * 1024; av[n][0] = ld_sc1(rws, lane * 16, so); av[n][1] = ld_sc1(rws, lane * 16, so + HST); }
      SCHED0();
    }
    STAMP(7);
    if (b == 0 && lw == 3) {
      const GAS char* hf = (const GAS char*)ws + WS_HFLAG + ((lane >> 3) * 8 + ((lane & 7) >> 1)) * 128 + (lane & 1) * 16;
      const GAS char* kf = (const GAS char*)ws + WS_KFLAG + (lane >> 4) * 512 + (lane & 15) * 8;
#pragma unroll 1
      for (unsigned spins = 0; spins < SPIN_CAP; ++spins) {
        const u64 v0 = __hip_atomic_load((const GAS u64*)hf, RLX_AGENT), v1 = __hip_atomic_load((const GAS u64*)(hf + 8192), RLX_AGENT);
        const u64 v2 = __hip_atomic_load((const GAS u64*)kf, RLX_AGENT), v3 = __hip_atomic_load((const GAS u64*)(kf + 2048), RLX_AGENT);
        if (__all(v0 == tag && v1 == tag && v2 == tag && v3 == tag)) break;
        __builtin_amdgcn_s_sleep(1);
      }
      if (lane == 0) __hip_atomic_store((GAS u64*)(ws + WS_EPOCH), tag, RLX_AGENT);
    }
    sy_wait(L, SY_CF + lw, 1);
  } else {
    const int m = lw & 1, nh = lw >> 1, q = lane >> 4;
    if (CPRIO) __builtin_amdgcn_s_setprio(CPRIO);
    if (lw == 0 && lane < 32) *(LAS unsigned*)(L + SYNC + lane * 4) = 0;
    if (PFM) {
      const int row = (xcd & 1) * 256 + r * 8;
      if (lw == 0) touch(pl, ((xcd & 1) * 1024 + (xcd >> 1) * 256 + r * 8 + (lane >> 3)) * 1024 + (lane & 7) * 128, RX);
      if (lw == 1) touch(pl, lane < 56 ? (row + lane / 7) * 3584 + (xcd >> 1) * 896 + (lane % 7) * 128 : OOB, RW1);
      if (lw == 2) touch(pl, lane < 48 ? (row + lane / 6) * 3072 + ((lane % 6) >> 1) * 1024 + (xcd >> 1) * 256 + (lane & 1) * 128 : OOB, RWQ);
      if (PFM > 1 && lw == 3) { touch(pl, lane < 56 ? (b * 56 + lane) * 128 : OOB, RW2); touch(pl, lane < 16 ? (b * 16 + lane) * 128 : OOB, RWO); }
    }
    if (PFC && lw == 0) touch(pl, lane < 8 ? (r * 8 + lane) * 128 : OOB, RSRC(pc0 & ~255ull, CODE_KB * 1024));
    if (PFT && lw == 1) {
      const unsigned o = lane < 6 ? WS_H + (lane < 4 ? ti * 4 + lane : t64 * 2 + (lane & 1)) * HST + 112 * 1024
                                  : (lane < 12 ? WS_O + (lane < 10 ? ti * 4 + (lane - 6) : t64 * 2 + (lane & 1)) * OST + 32 * 1024 : (lane == 12 ? 2048 : OOB));
      touch(pl, o, rws);
      touch(pl, lane == 0 ? (t64 * 64 * 512 + jc * 64) * 2 : OOB, RSRC(out, 2048 * 512 * 2));
    }
    tag = load_epoch(ws);
    GAS u64* own_flag;
    const int fA = 2 * ((lane >> 3) & 1) + (q & 1), rbA = 8 * q + ((lane & 15) >> 2);
    const int ax0 = slot_base(0) + m * 8192 + (lane & 15) * 128 + ((q ^ ((lane & 15) >> 1)) * 16);
    int d0 = slot_base(1) - slot_base(0), d1 = slot_base(2) - slot_base(1), d2 = slot_base(0) - slot_base(2);
    if (ffn) {
      LAS char *px0 = L + ax0, *px1 = L + (ax0 ^ 64), *pw[4], *ps;
      for (int i = 0; i < 4; i++) { const int T = 6 * nh + i; pw[i] = L + slot_base(0) + OFF_WM + rbA * 128 + (lane & 3) * 8 + (((T & 3) ^ fA) * 32) + (T >> 2) * 8192; }
      ps = L + slot_base(0) + OFF_WS + rbA * 64 + ((nh ^ (q & 1)) * 32) + (lane & 3) * 8;
      f32x4 ca[7][4];
      for (int i = 0; i < 7; i++) for (int j = 0; j < 4; j++) ca[i][j] = f32x4{0, 0, 0, 0};
      s16x8 xf[2][4], wf[2][7];
      auto rd = [&](int u, int bi) {
#pragma unroll
        for (int t = 0; t < 4; t++) xf[bi][t] = *(LAS s16x8*)((u ? px1 : px0) + t * 2048);
#pragma unroll
        for (int i = 0; i < 6; i++) wf[bi][i] = trfrag(pw[i & 3], u * 4096 + (i >> 2) * 8192, u * 4096 + 512 + (i >> 2) * 8192);
        wf[bi][6] = trfrag(ps, u * 2048, u * 2048 + 256);
      };
      auto mm = [&](int bi) {
#pragma unroll
        for (int i = 0; i < 7; i++)
#pragma unroll
          for (int t = 0; t < 4; t++) ca[i][t] = mfma16(wf[bi][i], xf[bi][t], ca[i][t]);
      };
      BAR();
      STAMP(1); STAMPC(0);
      rd(0, 0);
#pragma unroll 1
      for (int s = 0; s < 7; s++) {
        rd(1, 1);
        mm(0);
        px0 += d0; px1 += d0; ps += d0;
#pragma unroll
        for (int i = 0; i < 4; i++) pw[i] += d0;
#pragma unroll
        for (int i = 0; i < 18; i++) { SGB(0x8, 1); SGB(0x100, 1); }
#pragma unroll
        for (int i = 0; i < 7; i++) { SGB(0x8, 1); SGB(0x2, 1); }
        SGB(0x8, 3);
        { const int t = d0; d0 = d1; d1 = d2; d2 = t; }
        asm volatile("" : "+v"(px0), "+v"(px1), "+v"(ps), "+v"(pw[0]), "+v"(pw[1]), "+v"(pw[2]), "+v"(pw[3]));
        SCHED0();
        BAR();
        rd(0, 0); mm(1);
#pragma unroll
        for (int i = 0; i < 18; i++) { SGB(0x8, 1); SGB(0x100, 1); }
        SGB(0x8, 10);
        SCHED0();
      }
      rd(1, 1); mm(0);
#pragma unroll
      for (int i = 0; i < 18; i++) { SGB(0x8, 1); SGB(0x100, 1); }
      SGB(0x8, 10);
      SCHED0();
      BAR();
      SCHED0();
      STAMP(2); STAMPC(1);
      if (EXIT_A) { mm(1); float sm_ = 0; for (int i = 0; i < 7; i++) for (int j = 0; j < 4; j++) sm_ += ca[i][j][0] + ca[i][j][3]; if (sm_ == 1.2345f) out[0] = 1; WAITV(0); STAMPC(2); STAMP(13); STAMP_FLUSH; return; }
      const int kt_main = 14 * cg + ((cg & 1) ? 2 : 0) + 6 * nh, kt_side = 14 * cg + ((cg & 1) ? 0 : 12) + nh;
      const unsigned hrow = WS_H + (ti * 4 + m * 2) * HST;
#pragma unroll
      for (int i = 0; i < 8; i++) {
        if (i < 7) {
#pragma unroll
          for (int t = 0; t < 4; t++) ca[i][t] = mfma16(wf[1][i], xf[1][t], ca[i][t]);
        }
        if (i > 0) {
          const int kt = (i - 1 < 6) ? kt_main + i - 1 : kt_side;
#pragma unroll
          for (int a = 0; a < 2; a++) {
            const v4u f = to_frag32(ca[i - 1][2 * a], ca[i - 1][2 * a + 1]);
            st_sc1(v4u{relu2(f[0]), relu2(f[1]), relu2(f[2]), relu2(f[3])}, rws, lane * 16, hrow + a * HST + kt * 1024);
          }
        }
        if (i > 0 && i < 7) {
#pragma unroll
          for (int t = 0; t < 4; t++) { SGB(0x8, 1); SGB(0x2, 5); }
        }
        SCHED0();
      }
      if (HOLD) sy_set(L, SY_ST + lw, 1);
      STAMP(3); STAMPC(2);
      own_flag = (GAS u64*)(ws + WS_HFLAG + ((ti * 2 + m) * 4 + (cg >> 1)) * 128 + ((cg & 1) * 2 + nh) * 8);
    } else {
      const int p = nh;
      LAS char *pxp0 = L + ax0 + p * 4096, *pxp1 = L + (ax0 ^ 64) + p * 4096, *pxn0 = L + ax0 + (1 - p) * 4096, *pxn1 = L + (ax0 ^ 64) + (1 - p) * 4096, *pw[2];
      for (int i = 0; i < 2; i++) pw[i] = L + slot_base(0) + OFF_WM + rbA * 128 + (lane & 3) * 8 + (((2 * p + (i ^ m)) ^ fA) * 32);
      LAS char* pg = L + GATE_OFF + q * 16;
      v4u idn[2], sel[2];
#pragma unroll
      for (int v = 0; v < 2; v++)
#pragma unroll
        for (int jj = 0; jj < 4; jj++) {
          const int F = 8 * q + 2 * jj, c = 16 * v + (lane & 15);
          idn[v][jj] = ((c == F) ? 0x3f80u : 0u) | ((c == F + 1) ? 0x3f800000u : 0u);
        }
      asm volatile("" : "+v"(idn[0]), "+v"(idn[1]));
      f32x4 cq[2][4], ck[2][4], cv[2][4], cgate[2];
      for (int i = 0; i < 2; i++) { cgate[i] = f32x4{0, 0, 0, 0}; for (int t = 0; t < 4; t++) { cq[i][t] = cgate[i]; ck[i][t] = cgate[i]; cv[i][t] = cgate[i]; } }
      s16x8 xf[2][4], wf[2][6], gf[2];
      auto rd = [&](int u, int bi) {
#pragma unroll
        for (int t = 0; t < 4; t++) xf[bi][t] = *(LAS s16x8*)((t < 2 ? (u ? pxp1 : pxp0) : (u ? pxn1 : pxn0)) + (t & 1) * 2048);
#pragma unroll
        for (int i = 0; i < 6; i++) { const int l = (i >> 1) == 2 ? 0 : (i >> 1) + 1; wf[bi][i] = trfrag(pw[i & 1], u * 4096 + l * 8192, u * 4096 + 512 + l * 8192); }
        gf[bi] = *(LAS s16x8*)(pg + u * 64);
      };
      auto mm = [&](int bi) {
#pragma unroll
        for (int i = 0; i < 2; i++)
#pragma unroll
          for (int t = 0; t < 4; t++) ck[i][t] = mfma16(wf[bi][i], xf[bi][t], ck[i][t]);
#pragma unroll
        for (int i = 0; i < 2; i++)
#pragma unroll
          for (int t = 0; t < 4; t++) cv[i][t] = mfma16(xf[bi][t], wf[bi][2 + i], cv[i][t]);
#pragma unroll
        for (int i = 0; i < 2; i++)
#pragma unroll
          for (int t = 0; t < 4; t++) cq[i][t] = mfma16(wf[bi][4 + i], xf[bi][t], cq[i][t]);
        cgate[0] = mfma16(gf[bi], xf[bi][0], cgate[0]);
        cgate[1] = mfma16(gf[bi], xf[bi][1], cgate[1]);
      };
      BAR();
      STAMP(1); STAMPC(0);
      rd(0, 0);
      v4u wp[2];
      { LAS char* wa = L + WPHI_OFF + 2 * p * 512 + (8 * hh + ((lane & 15) >> 2)) * 32 + (8 * ((lane >> 4) & 1) + 4 * ((lane & 3) >> 1)) * 2;
        const unsigned sg = ((lane >> 2) & 1) ? 0x80008000u : 0u;
        wp[0] = __builtin_bit_cast(v4u, trfrag(wa + m * 512, 0, 128)) ^ sg;
        wp[1] = __builtin_bit_cast(v4u, trfrag(wa + (1 - m) * 512, 0, 128)) ^ sg;
        asm volatile("" : "+v"(wp[0]), "+v"(wp[1])); }
      SCHED0();
#pragma unroll 1
      for (int s = 0; s < 7; s++) {
        rd(1, 1);
        mm(0);
        pxp0 += d0; pxp1 += d0; pxn0 += d0; pxn1 += d0; pw[0] += d0; pw[1] += d0; pg += 128;
#pragma unroll
        for (int i = 0; i < 17; i++) { SGB(0x8, 1); SGB(0x100, 1); }
#pragma unroll
        for (int i = 0; i < 7; i++) { SGB(0x8, 1); SGB(0x2, 1); }
        SGB(0x8, 2);
        { const int t = d0; d0 = d1; d1 = d2; d2 = t; }
        asm volatile("" : "+v"(pxp0), "+v"(pxp1), "+v"(pxn0), "+v"(pxn1), "+v"(pw[0]), "+v"(pw[1]), "+v"(pg));
        SCHED0();
        BAR();
        rd(0, 0); mm(1);
#pragma unroll
        for (int i = 0; i < 17; i++) { SGB(0x8, 1); SGB(0x100, 1); }
        SGB(0x8, 9);
        SCHED0();
      }
      rd(1, 1); mm(0);
#pragma unroll
      for (int i = 0; i < 17; i++) { SGB(0x8, 1); SGB(0x100, 1); }
      SGB(0x8, 9);
      SCHED0();
      BAR();
      SCHED0();
      STAMP(2); STAMPC(1);
      if (EXIT_A) { mm(1); float sm_ = cgate[0][0] + cgate[1][0]; for (int i = 0; i < 2; i++) for (int j = 0; j < 4; j++) sm_ += cq[i][j][0] + ck[i][j][0] + cv[i][j][0]; if (sm_ == 1.2345f) out[0] = 1; WAITV(0); STAMPC(2); STAMP(13); STAMP_FLUSH; return; }
      f32x4 kv[2][2], zz[2][2];
      {
        f32x16 P[4]; v4u ph[4][2], vb[4];
        mm(1);
#pragma unroll
        for (int u = 0; u < 4; u++) P[u] = mfma32(wp[u & 1], to_frag32(ck[u & 1][u & 2], ck[u & 1][(u & 2) + 1]), zero16());
#pragma unroll
        for (int u = 0; u < 4; u++) vb[u] = pack44(cv[u & 1][u & 2], cv[u & 1][(u & 2) + 1]);
        SGB(0x8, 10);
#pragma unroll
        for (int i = 0; i < 8; i++) { SGB(0x8, 1); SGB(0x2, 3); }
#pragma unroll
        for (int i = 0; i < 8; i++) { SGB(0x8, 1); SGB(0x2, 2); }
        SGB(0x8, 4);
        SCHED0();
        STAMPC(3);
        const v4u ones = {0x3f803f80u, 0x3f803f80u, 0x3f803f80u, 0x3f803f80u};
        const f32x4 z4 = {0, 0, 0, 0};
        f32x4 Z[4][2][2];
        auto transp = [&](int u) {
          v4u a0, a1;
#pragma unroll
          for (int i = 0; i < 4; i++) { const auto sw_ = __builtin_amdgcn_permlane16_swap(ph[u][0][i], ph[u][1][i], false, false); a0[i] = sw_[0]; a1[i] = sw_[1]; }
#pragma unroll
          for (int v = 0; v < 2; v++) { Z[u][0][v] = mfma16(a0, idn[v], z4); Z[u][1][v] = mfma16(a1, idn[v], z4); }
        };
        auto accum = [&](int u) {
#pragma unroll
          for (int v = 0; v < 2; v++) {
            const v4u a = pack44(Z[u][0][v], Z[u][1][v]);
            kv[u & 1][v] = mfma16(a, vb[u], u < 2 ? z4 : kv[u & 1][v]);
            zz[u & 1][v] = mfma16(a, ones, u < 2 ? z4 : zz[u & 1][v]);
          }
        };
        softmax16<2>(P, ph);
        transp(0); transp(1);
        SCHED0();
        STAMPC(4);
        accum(0); accum(1);
        softmax16<2>(P + 2, ph + 2);
        transp(3); transp(2);
        SCHED0();
        accum(3); accum(2);
      }
      STAMPC(5);
      const int hd = 4 * cg + 2 * p + m;
      {
        LAS char* xw = L + SCR_XCH + lw * 2176;
        *(LAS f32x4*)(xw + lane * 16) = kv[1][0];
        *(LAS f32x4*)(xw + 1024 + lane * 16) = kv[1][1];
        if ((lane & 15) == 0) { *(LAS f32x4*)(xw + 2048 + q * 32) = zz[1][0]; *(LAS f32x4*)(xw + 2048 + q * 32 + 16) = zz[1][1]; }
        if (lane < 16) { *(LAS float*)(L + SCR_GX + lw * 128 + lane * 4) = cgate[0][0]; *(LAS float*)(L + SCR_GX + lw * 128 + 64 + lane * 4) = cgate[1][0]; }
        sy_set(L, SY_XCH + lw, 1);
        STAMPC(6);
        sy_wait(L, SY_XCH + (lw ^ 1), 1);
        STAMPC(7);
        const LAS char* xr = L + SCR_XCH + (lw ^ 1) * 2176;
        const f32x4 k0 = kv[0][0] + *(const LAS f32x4*)(xr + lane * 16), k1 = kv[0][1] + *(const LAS f32x4*)(xr + 1024 + lane * 16);
        const f32x4 y0 = zz[0][0] + *(const LAS f32x4*)(xr + 2048 + q * 32), y1 = zz[0][1] + *(const LAS f32x4*)(xr + 2048 + q * 32 + 16);
        st_sc1(pack44(k0, k1), rws, lane * 16, WS_KVP + (hd * 16 + ti) * 1024);
        st_sc1(pack44(y0, y1), rws, (lane & 15) == 0 ? q * 16 : OOB, WS_ZP + (hd * 16 + ti) * 128);
      }
      SCHED0();
      STAMP(3); STAMPC(2);
      v4u pq[4][2];
      {
        f32x16 P[4];
#pragma unroll
        for (int u = 0; u < 4; u++) P[u] = mfma32(wp[u & 1], to_frag32(cq[u & 1][u & 2], cq[u & 1][(u & 2) + 1]), zero16());
#pragma unroll
        for (int u = 0; u < 2; u++)
#pragma unroll
          for (int jj = 0; jj < 4; jj++) {
            const int f0 = 16 * (jj >> 1) + 4 * q + 2 * (jj & 1), rho = lane & 15;
            sel[u][jj] = ((f0 == 16 * u + rho) ? 0x3f80u : 0u) | ((f0 + 1 == 16 * u + rho) ? 0x3f800000u : 0u);
          }
        asm volatile("" : "+v"(sel[0]), "+v"(sel[1]));
        softmax16<1>(P, pq);
        asm volatile("" : "+v"(pq[0][0]), "+v"(pq[0][1]), "+v"(P[1]), "+v"(P[2]), "+v"(P[3]));
        WAITV(0);
        if (lane == 0) __hip_atomic_store((GAS u64*)(ws + WS_KFLAG + hd * 128 + ti * 8), tag, RLX_AGENT);
        STAMP(4);
        asm volatile("" : "+v"(P[1]), "+v"(P[2]), "+v"(P[3]));
        softmax16<3>(P + 1, pq + 1);
#pragma unroll
        for (int u = 1; u < 4; u++) asm volatile("" : "+v"(pq[u][0]), "+v"(pq[u][1]));
      }
      sy_wait(L, SY_XCH + (lw ^ 2), 1);
      float gt[2];
      { const float l0 = *(const LAS float*)(L + SCR_GX + lw * 128 + (lane & 31) * 4), l1 = *(const LAS float*)(L + SCR_GX + (lw ^ 2) * 128 + (lane & 31) * 4);
        gt[0] = __builtin_amdgcn_rcpf(1.f + __builtin_amdgcn_exp2f(-1.4426950408889634f * l0));
        gt[1] = __builtin_amdgcn_rcpf(1.f + __builtin_amdgcn_exp2f(-1.4426950408889634f * l1));
        asm volatile("" : "+v"(gt[0]), "+v"(gt[1])); }
      STAMP(5);
      {
        const int rho = lane & 15;
        poll_flags((const GAS u64*)(ws + WS_KFLAG + hd * 128 + (lane & 15) * 8), tag);
        STAMP(6);
        const v4u zp = ld_sc1(rws, (lane & 15) * 128 + q * 16, WS_ZP + hd * 2048);
        v4u kp[16];
#pragma unroll
        for (int i = 0; i < 16; i++) kp[i] = ld_sc1(rws, lane * 16, WS_KVP + (hd * 16 + ((i + ti) & 15)) * 1024);
        SCHED0();
        LAS char* ks = L + SCR_KS + lw * 1088;
        float z[8];
#pragma unroll
        for (int jj = 0; jj < 4; jj++) { z[2 * jj] = row_sum16(__uint_as_float(zp[jj] << 16)); z[2 * jj + 1] = row_sum16(__uint_as_float(zp[jj] & 0xffff0000u)); }
        if (rho == 0) {
          *(LAS v2u*)(ks + 1024 + q * 8) = v2u{pk(z[0], z[1]), pk(z[2], z[3])};
          *(LAS v2u*)(ks + 1024 + q * 8 + 32) = v2u{pk(z[4], z[5]), pk(z[6], z[7])};
        }
        SCHED0();
        f32x4 t0 = {0, 0, 0, 0}, t1 = {0, 0, 0, 0};
#pragma unroll
        for (int i = 0; i < 16; i++) { t0 = mfma16(sel[0], kp[i], t0); t1 = mfma16(sel[1], kp[i], t1); }
        *(LAS v2u*)(ks + rho * 64 + q * 8) = v2u{pk(t0[0], t0[1]), pk(t0[2], t0[3])};
        *(LAS v2u*)(ks + rho * 64 + 32 + q * 8) = v2u{pk(t1[0], t1[1]), pk(t1[2], t1[3])};
        sy_set(L, SY_KS + lw, 1);
      }
      STAMP(7);
      { const int rho = lane & 31, krow = rho < 16 ? (8 * ((rho >> 2) & 1) + (rho & 3) + 4 * (rho >> 3)) : 16;
        const unsigned orow = WS_O + (ti * 4 + m * 2) * OST + (4 * cg + 2 * p) * 1024;
#pragma unroll
        for (int s = 0; s < 2; s++) {
          if (s) sy_wait(L, SY_KS + (lw ^ 1), 1);
          const LAS char* ks = L + SCR_KS + (lw ^ s) * 1088 + krow * 64 + hh * 32;
          const v4u k0 = *(const LAS v4u*)ks, k1 = *(const LAS v4u*)(ks + 16);
          f32x16 o[2];
#pragma unroll
          for (int t = 0; t < 2; t++) { o[t] = mfma32(k0, pq[2 * t + s][0], zero16()); o[t] = mfma32(k1, pq[2 * t + s][1], o[t]); }
#pragma unroll
          for (int t = 0; t < 2; t++) {
            const float sc = gt[t] * __builtin_amdgcn_rcpf(o[t][8] + 1e-6f);
#pragma unroll
            for (int i = 0; i < 8; i++) o[t][i] *= sc;
            st_sc1(pack8(o[t], 0), rws, lane * 16, orow + (t ^ p) * OST + (s ^ m) * 1024);
          }
        }
        const unsigned go = ((cg & 1) == 0 && p == 0) ? (WS_G + ((ti * 2 + m) * 4 + (cg >> 1)) * 256 + lane * 4) : OOB;
        __builtin_amdgcn_raw_buffer_store_b32(__float_as_uint(1.f - (hh ? gt[1] : gt[0])), rws, go, 0, 16);
        own_flag = (GAS u64*)(ws + WS_OFLAG + ((ti * 2 + m) * 4 + (cg >> 1)) * 128 + ((cg & 1) * 2 + p) * 8);
      }
    }
    publish_then_poll(own_flag, (const GAS u64*)(ws + hflag_off), tag, lane);
    STAMP(8);
    v4u av[10][2];
#pragma unroll
    for (int j = 0; j < 10; j++) { const unsigned so = hbase + ktf(18 + j) * 1024; av[j][0] = ld_sc1(rws, lane * 16, so); av[j][1] = ld_sc1(rws, lane * 16, so + HST); }
    SCHED0();
    sy_wait(L, SY_WA + lw, 1);
    acc[0][0] = zero16(); acc[0][1] = zero16(); acc[1][0] = zero16(); acc[1][1] = zero16();
#pragma unroll
    for (int j = 0; j < 10; j++) {
      const s16x8 w0 = trfrag(L + zbase(j) + awB0, zoffs(j), zoffs(j) + 512), w1f = trfrag(L + zbase(j) + awB1, zoffs(j), zoffs(j) + 512);
      acc[0][0] = mfma32(w0, av[j][0], acc[0][0]);
      acc[1][0] = mfma32(w1f, av[j][0], acc[1][0]);
      acc[0][1] = mfma32(w0, av[j][1], acc[0][1]);
      acc[1][1] = mfma32(w1f, av[j][1], acc[1][1]);
    }
    SCHED0();
    sy_set(L, SY_CF + lw, 1);
    STAMP(9);
    sy_wait(L, SY_WB + lw, 1);
    s16x8 wa[8][2];
#pragma unroll
    for (int j = 0; j < 8; j++) { wa[j][0] = trfrag(L + zbase(10 + j) + awB0, zoffs(10 + j), zoffs(10 + j) + 512); wa[j][1] = trfrag(L + zbase(10 + j) + awB1, zoffs(10 + j), zoffs(10 + j) + 512); }
    SCHED0();
    poll_flags((const GAS u64*)(ws + hflag_off + (WS_OFLAG - WS_HFLAG)), tag);
    STAMP(10);
    v4u ov[8][2];
    const unsigned go = WS_G + (t64 * 4 + lw) * 256;
    const float g0 = __uint_as_float(__builtin_amdgcn_raw_buffer_load_b32(rws, (lane & 31) * 4, go, 16));
    const float g1 = __uint_as_float(__builtin_amdgcn_raw_buffer_load_b32(rws, (lane & 31) * 4, go + 128, 16));
    const unsigned obase = WS_O + t64 * 2 * OST + 8 * lw * 1024;
#pragma unroll
    for (int j = 0; j < 8; j++) { const unsigned so = obase + ((j + rotc) & 7) * 1024; ov[j][0] = ld_sc1(rws, lane * 16, so); ov[j][1] = ld_sc1(rws, lane * 16, so + OST); }
    SCHED0();
    if (lw == 0) *(LAS float*)(L + SCR_G2 + lane * 4) = hh ? g1 : g0;
#pragma unroll
    for (int i = 0; i < 16; i++) { acc[0][0][i] *= g0; acc[1][0][i] *= g0; acc[0][1][i] *= g1; acc[1][1][i] *= g1; }
    SCHED0();
#pragma unroll
    for (int j = 0; j < 8; j++) {
      acc[0][0] = mfma32(wa[j][0], ov[j][0], acc[0][0]);
      acc[1][0] = mfma32(wa[j][1], ov[j][0], acc[1][0]);
      acc[0][1] = mfma32(wa[j][0], ov[j][1], acc[0][1]);
      acc[1][1] = mfma32(wa[j][1], ov[j][1], acc[1][1]);
    }
  }
  STAMP(11);
  {
    const int half = wid >> 2;
    LAS char* wa_ = L + lane * 16 + (half ? zb2 : zb3);
    LAS char* wb_ = L + lane * 16 + zb4;
#pragma unroll
    for (int d = 0; d < 4; d++) {
      const f32x16& t = acc[d >> 1][d & 1];
#pragma unroll
      for (int i = 0; i < 4; i++) {
        const int n = d * 4 + i;
        LAS char* w = half ? (n < 10 ? wa_ + n * 1024 : L + lane * 16 + zb1 + (n - 10) * 1024) : (n < 8 ? wa_ + n * 1024 : wb_ + (n - 8) * 1024);
        *(LAS f32x4*)w = f32x4{t[4 * i], t[4 * i + 1], t[4 * i + 2], t[4 * i + 3]};
      }
    }
    BAR();
    STAMP(12);
    const float g = *(const LAS float*)(L + SCR_G2 + (lw & 1) * 128 + (lane & 31) * 4);
    const int n0 = lw * 4 + half * 2;
    const LAS char* rc = L + lane * 16 + (n0 < 8 ? 33 * BLK + n0 * 1024 : 57 * BLK + (n0 - 8) * 1024);
    const LAS char* rl = L + lane * 16 + (n0 < 10 ? 13 * BLK + n0 * 1024 : 1 * BLK + (n0 - 10) * 1024);
    const int sc = n0 < 8 ? 6 * BLK : 4 * BLK, sl = n0 < 10 ? 5 * BLK : 3 * BLK;
    f32x4 c0 = *(const LAS f32x4*)rc, c1 = *(const LAS f32x4*)(rc + 1024), l0 = *(const LAS f32x4*)rl, l1 = *(const LAS f32x4*)(rl + 1024);
#pragma unroll
    for (int i = 1; i < 4; i++) {
      c0 += *(const LAS f32x4*)(rc + i * sc); c1 += *(const LAS f32x4*)(rc + i * sc + 1024);
      l0 += *(const LAS f32x4*)(rl + i * sl); l1 += *(const LAS f32x4*)(rl + i * sl + 1024);
    }
    c0 += l0 * g; c1 += l1 * g;
    const unsigned a0 = pk(c0[0], c0[1]), a1 = pk(c0[2], c0[3]), b0 = pk(c1[0], c1[1]), b1 = pk(c1[2], c1[3]);
    const auto r0 = __builtin_amdgcn_permlane32_swap(a0, b0, false, false);
    const auto r1 = __builtin_amdgcn_permlane32_swap(a1, b1, false, false);
    const int tok = t64 * 64 + (lw & 1) * 32 + (lane & 31);
    const int col = jc * 64 + (lw >> 1) * 32 + half * 16 + hh * 8;
    __builtin_amdgcn_raw_buffer_store_b128(v4u{r0[0], r1[0], r0[1], r1[1]}, RSRC(out, 2048 * 512 * 2), (tok * 512 + col) * 2, 0, OUTSC);
  }
  STAMP(13);
  STAMP_FLUSH;
}

#define DIAG_LAUNCH

extern "C" void kernel_launch(void* const* d_in, const int* in_sizes, int n_in, void* d_out, int out_size, void* d_ws, size_t ws_size,
                              hipStream_t stream) {
  (void)hipFuncSetAttribute((const void*)fused_kernel, hipFuncAttributeMaxDynamicSharedMemorySize, LDS_BYTES);
  fused_kernel<<<256, 512, LDS_BYTES, stream>>>((const u16*)d_in[0], (const u16*)d_in[1], (const u16*)d_in[2], (const u16*)d_in[3],
                                                (const u16*)d_in[5], (const u16*)d_in[6], (const u16*)d_in[7], (u16*)d_out, (char*)d_ws);
  DIAG_LAUNCH
}
```

```cpp
#include <hip/hip_runtime.h>
#include <hip/hip_bf16.h>
#include <type_traits>

typedef unsigned short u16;
typedef unsigned long long u64;
typedef unsigned v4u __attribute__((ext_vector_type(4)));
typedef unsigned v2u __attribute__((ext_vector_type(2)));
typedef short s16x2 __attribute__((ext_vector_type(2)));
typedef short s16x4 __attribute__((ext_vector_type(4)));
typedef short s16x8 __attribute__((ext_vector_type(8)));
typedef __bf16 bf16x8 __attribute__((ext_vector_type(8)));
typedef __bf16 bf16x2 __attribute__((ext_vector_type(2)));
typedef float f32x2 __attribute__((ext_vector_type(2)));
typedef float f32x4 __attribute__((ext_vector_type(4)));
typedef float f32x16 __attribute__((ext_vector_type(16)));
typedef __amdgpu_buffer_rsrc_t rsrc_t;
#define LAS __attribute__((address_space(3)))
#define GAS __attribute__((address_space(1)))
#define DI __device__ __forceinline__
#define RLX_AGENT __ATOMIC_RELAXED, __HIP_MEMORY_SCOPE_AGENT

constexpr int POLLS = 1;
constexpr int JSWAP = 4;
constexpr int LW0 = 6;
constexpr int GWIN = 12;
constexpr int LWQ = 3;
constexpr int GROW = 3;
constexpr int JMIX = 0;
constexpr int ADEF = 5;
constexpr int CODE_LINES = 349;
constexpr int PFCW = 0;

constexpr unsigned WS_EPOCH = 0;
constexpr unsigned WS_HFLAG = 4096;
constexpr unsigned WS_OFLAG = WS_HFLAG + 128 * 128;
constexpr unsigned WS_KFLAG = WS_OFLAG + 128 * 128;
constexpr unsigned WS_G     = WS_KFLAG + 32 * 128;
constexpr unsigned WS_ZP    = WS_G + 128 * 256;
constexpr unsigned WS_KVP   = WS_ZP + 512 * 128;
constexpr unsigned WS_O     = WS_KVP + 512 * 1024;
constexpr unsigned OST = 33 * 1024, HST = 113 * 1024;
constexpr unsigned WS_H     = WS_O + 64 * OST;
constexpr unsigned WS_END   = WS_H + 64 * HST;
constexpr unsigned OOB = 0x70000000u;

constexpr int BLK = 2048;
constexpr int OFF_WM = 16384, OFF_WS = 40960;
constexpr int LDS_BYTES = 163840;
__host__ __device__ constexpr int slot_base(int i) { return i == 0 ? 36 * BLK : (i == 1 ? 58 * BLK : 14 * BLK); }
constexpr int GATE_OFF = 0;
constexpr int SYNC = 1024;
constexpr int SY_XR = 0, SY_DONE = 4, SY_XCH = 8, SY_KS = 12, SY_WA = 16, SY_WB = 20, SY_CF = 24;
constexpr int PF_OFF = 1280;
constexpr int TAG_OFF = 1536;
constexpr int WPHI_OFF = slot_base(0) + OFF_WS;
constexpr int SCR = 73 * BLK;
constexpr int SCR_KS = SCR;
constexpr int SCR_GX = SCR_KS + 4352;
constexpr int SCR_G2 = SCR_GX + 512;
constexpr unsigned SPIN_CAP = 1u << 16;

DI void dma(unsigned lds, int imm, unsigned voff, rsrc_t r, unsigned soff) {
  asm volatile("s_add_i32 m0, %0, %4\n\ts_nop 0\n\tbuffer_load_dwordx4 %1, %2, %3 offen lds" ::"s"(lds), "v"(voff), "s"(r), "s"(soff), "i"(imm) : "memory", "scc");
}
DI void touch(unsigned lds, unsigned voff, rsrc_t r) {
  asm volatile("s_mov_b32 m0, %0\n\ts_nop 0\n\tbuffer_load_dword %1, %2, 0 offen lds" ::"s"(lds), "v"(voff), "s"(r) : "memory");
}
#define OPAQUE_S(x) asm volatile("" : "+s"(x))
DI s16x8 trfrag(LAS char* p, int off0, int off1) {
  s16x4 a = __builtin_amdgcn_ds_read_tr16_b64_v4i16((LAS s16x4*)(p + off0));
  s16x4 b = __builtin_amdgcn_ds_read_tr16_b64_v4i16((LAS s16x4*)(p + off1));
  return __builtin_shufflevector(a, b, 0, 1, 2, 3, 4, 5, 6, 7);
}
template <class A, class B> DI f32x4 mfma16(A a, B b, f32x4 c) {
  return __builtin_amdgcn_mfma_f32_16x16x32_bf16(__builtin_bit_cast(bf16x8, a), __builtin_bit_cast(bf16x8, b), c, 0, 0, 0);
}
template <class A, class B> DI f32x16 mfma32(A a, B b, f32x16 c) {
  return __builtin_amdgcn_mfma_f32_32x32x16_bf16(__builtin_bit_cast(bf16x8, a), __builtin_bit_cast(bf16x8, b), c, 0, 0, 0);
}
DI unsigned pk(float a, float b) { f32x2 v = {a, b}; return __builtin_bit_cast(unsigned, __builtin_convertvector(v, bf16x2)); }
DI unsigned relu2(unsigned v) { return __builtin_bit_cast(unsigned, __builtin_elementwise_max(__builtin_bit_cast(s16x2, v), s16x2{0, 0})); }
DI v4u pack44(const f32x4& a, const f32x4& b) { return v4u{pk(a[0], a[1]), pk(a[2], a[3]), pk(b[0], b[1]), pk(b[2], b[3])}; }
DI v4u to_frag32(const f32x4& c0, const f32x4& c1) {
  const auto s0 = __builtin_amdgcn_permlane16_swap(pk(c0[0], c0[1]), pk(c1[0], c1[1]), false, false);
  const auto s1 = __builtin_amdgcn_permlane16_swap(pk(c0[2], c0[3]), pk(c1[2], c1[3]), false, false);
  return v4u{s0[0], s1[0], s0[1], s1[1]};
}
DI v4u pack8(const f32x16& x, int s) {
  return v4u{pk(x[8 * s], x[8 * s + 1]), pk(x[8 * s + 2], x[8 * s + 3]), pk(x[8 * s + 4], x[8 * s + 5]), pk(x[8 * s + 6], x[8 * s + 7])};
}
DI f32x16 zero16() { f32x16 z; for (int i = 0; i < 16; i++) z[i] = 0.f; return z; }
#define WAITV(n) asm volatile("s_waitcnt vmcnt(" #n ")" ::: "memory")
#define WAITL() asm volatile("s_waitcnt lgkmcnt(0)" ::: "memory")
#define BAR() do { asm volatile("s_waitcnt lgkmcnt(0)" ::: "memory"); __builtin_amdgcn_s_barrier(); asm volatile("" ::: "memory"); } while (0)
#define SCHED0() __builtin_amdgcn_sched_barrier(0)
#define SGB(mask, n) __builtin_amdgcn_sched_group_barrier(mask, n, 0)
DI v4u ld_sc1(rsrc_t r, unsigned voff, unsigned soff) { return __builtin_amdgcn_raw_buffer_load_b128(r, voff, soff, 16); }
DI void st_sc1(v4u v, rsrc_t r, unsigned voff, unsigned soff) {
  __builtin_amdgcn_raw_buffer_store_b128(v, r, voff, soff, 16);
  asm volatile("s_nop 1" ::"v"(v) : "memory");
}

DI void poll_flags(const GAS u64* p, u64 tag) {
#pragma unroll 1
  for (unsigned spins = 0; spins < SPIN_CAP; ++spins) {
    u64 v = __hip_atomic_load(p, RLX_AGENT);
    if (__all(v == tag)) break;
    if (POLLS) __builtin_amdgcn_s_sleep(POLLS);
  }
  __builtin_amdgcn_fence(__ATOMIC_ACQUIRE, "wavefront");
  asm volatile("" ::: "memory");
}
DI void publish_then_poll(GAS u64* own, const GAS u64* p, u64 tag, int lane) {
  u64 v = __hip_atomic_load(p, RLX_AGENT);
  asm volatile("s_waitcnt vmcnt(0)" : "+v"(v) :: "memory");
  if (lane == 0) __hip_atomic_store(own, tag, RLX_AGENT);
  if (!__all(v == tag)) poll_flags(p, tag);
  __builtin_amdgcn_fence(__ATOMIC_ACQUIRE, "wavefront");
  asm volatile("" ::: "memory");
}
DI v2u epoch_load(const char* ws) {
  v2u e;
  asm volatile("global_load_dwordx2 %0, %1, %2 sc1" : "=v"(e) : "v"(0u), "s"(ws) : "memory");
  return e;
}
DI u64 epoch_tag(LAS char* L) {
  const v2u e = *(volatile LAS v2u*)(L + TAG_OFF);
  return (((u64)(unsigned)__builtin_amdgcn_readfirstlane(e[1]) << 32) | (unsigned)__builtin_amdgcn_readfirstlane(e[0])) + 1;
}
DI float row_sum16(float x) {
  x += __uint_as_float(__builtin_amdgcn_mov_dpp(__float_as_uint(x), 0x128, 0xf, 0xf, true));
  x += __uint_as_float(__builtin_amdgcn_mov_dpp(__float_as_uint(x), 0x124, 0xf, 0xf, true));
  x += __uint_as_float(__builtin_amdgcn_mov_dpp(__float_as_uint(x), 0x122, 0xf, 0xf, true));
  x += __uint_as_float(__builtin_amdgcn_mov_dpp(__float_as_uint(x), 0x121, 0xf, 0xf, true));
  return x;
}
DI unsigned sy_read(LAS char* L, int w) { return *(volatile LAS unsigned*)(L + SYNC + w * 4); }
DI void sy_wait(LAS char* L, int w, unsigned need) {
#pragma unroll 1
  for (unsigned spins = 0; spins < (SPIN_CAP << 6); ++spins)
    if (__builtin_amdgcn_readfirstlane(sy_read(L, w)) >= need) break;
  asm volatile("" ::: "memory");
}
DI void sy_wait_s(LAS char* L, int w, unsigned need) {
#pragma unroll 1
  for (unsigned spins = 0; spins < (SPIN_CAP << 4); ++spins) {
    if (__builtin_amdgcn_readfirstlane(sy_read(L, w)) >= need) break;
    __builtin_amdgcn_s_sleep(2);
  }
  asm volatile("" ::: "memory");
}
DI void sy_wait4_s(LAS char* L, int w) {
#pragma unroll 1
  for (unsigned spins = 0; spins < (SPIN_CAP << 4); ++spins) {
    const v4u v = *(volatile LAS v4u*)(L + SYNC + w * 4);
    if (__builtin_amdgcn_readfirstlane(v[0] & v[1] & v[2] & v[3])) break;
    __builtin_amdgcn_s_sleep(1);
  }
  asm volatile("" ::: "memory");
}
DI void sy_set(LAS char* L, int w, unsigned v) {
  asm volatile("s_waitcnt lgkmcnt(0)" ::: "memory");
  if ((threadIdx.x & 63) == 0) *(volatile LAS unsigned*)(L + SYNC + w * 4) = v;
}
struct NoHook { DI void operator()() const {} };
template <int N, class H = NoHook> DI void softmax16(f32x16* p, v4u (*o)[2], H hook = H()) {
  const float L2E = 1.4426950408889634f;
  float m[N]; f32x2 e[N][8], s[N];
  float ninf = -__builtin_inff();
  asm volatile("" : "+s"(ninf));
#pragma unroll
  for (int i = 0; i < 16; i += 2)
#pragma unroll
    for (int u = 0; u < N; u++) m[u] = fmaxf(fmaxf(i ? m[u] : ninf, p[u][i]), p[u][i + 1]);
#pragma unroll
  for (int u = 0; u < N; u++) m[u] *= -L2E;
  hook();
#pragma unroll
  for (int i = 0; i < 8; i++)
#pragma unroll
    for (int u = 0; u < N; u++) {
      const f32x2 t = __builtin_elementwise_fma(f32x2{p[u][2 * i], p[u][2 * i + 1]}, f32x2{L2E, L2E}, f32x2{m[u], m[u]});
      e[u][i] = f32x2{__builtin_amdgcn_exp2f(t[0]), __builtin_amdgcn_exp2f(t[1])};
      s[u] = i ? s[u] + e[u][i] : e[u][i];
    }
#pragma unroll
  for (int u = 0; u < N; u++) {
    const float r = __builtin_amdgcn_rcpf(s[u][0] + s[u][1]);
#pragma unroll
    for (int i = 0; i < 8; i++) { const f32x2 t = e[u][i] * r; o[u][i >> 2][i & 3] = pk(t[0], t[1]); }
  }
}

DI void make_idn(v4u (&idn)[2], int lane) {
#pragma unroll
  for (int v = 0; v < 2; v++)
#pragma unroll
    for (int jj = 0; jj < 4; jj++) {
      const int F = 8 * (lane >> 4) + 2 * jj, c = 16 * v + (lane & 15);
      idn[v][jj] = ((c == F) ? 0x3f80u : 0u) | ((c == F + 1) ? 0x3f800000u : 0u);
    }
  asm volatile("" : "+v"(idn[0]), "+v"(idn[1]));
}
DI v4u make_wp(LAS char* w, int lane) {
  LAS char* wa = w + (8 * (lane >> 5) + ((lane & 15) >> 2)) * 32 + (8 * ((lane >> 4) & 1) + 4 * ((lane & 3) >> 1)) * 2;
  v4u r = __builtin_bit_cast(v4u, trfrag(wa, 0, 128)) ^ (((lane >> 2) & 1) ? 0x80008000u : 0u);
  asm volatile("" : "+v"(r));
  return r;
}
__global__ __launch_bounds__(512, 2) void fused_kernel(const u16* __restrict__ x, const u16* __restrict__ wqkv, const u16* __restrict__ wphi,
                                                       const u16* __restrict__ wo, const u16* __restrict__ w1, const u16* __restrict__ w2,
                                                       const u16* __restrict__ gw, u16* __restrict__ out, char* __restrict__ ws) {
  extern __shared__ __attribute__((aligned(16))) char shm[];
  LAS char* L = (LAS char*)shm;
  const unsigned ldsbase = (unsigned)(size_t)L;

  const int tid = threadIdx.x, lane = tid & 63;
  const int wid = __builtin_amdgcn_readfirstlane(tid >> 6);
  const int lw = wid & 3;
  const int b = blockIdx.x, xcd = b & 7, r = b >> 3;
  if (wid == PFCW) {
    u64 pc0;
    asm volatile("s_getpc_b64 %0" : "=s"(pc0));
    constexpr int NL = (CODE_LINES + 31) / 32;
    touch(ldsbase + PF_OFF, lane < NL ? (r * NL + lane) * 128 : OOB, __builtin_amdgcn_make_buffer_rsrc((void*)(pc0 & ~255ull), (short)0, CODE_LINES * 128, 0x00020000));
  }
  SCHED0();
  const bool ffn = (r >> 4) == 0;
  const int ti = (xcd & 1) * 8 + (r & 7), cg = (xcd >> 1) * 2 + ((r >> 3) & 1);
#define RSRC(p, bytes) __builtin_amdgcn_make_buffer_rsrc((void*)(p), (short)0, (bytes), 0x00020000)
#define RX RSRC(x, 2048 * 512 * 2)
#define RW1 RSRC(w1, 512 * 1792 * 2)
#define RWQ RSRC(wqkv, 512 * 1536 * 2)
#define RW2 RSRC(w2, 1792 * 512 * 2)
#define RWO RSRC(wo, 512 * 512 * 2)

  const rsrc_t rx = RX;
  const rsrc_t rw = RSRC(ffn ? w1 : wqkv, ffn ? 512 * 1792 * 2 : 512 * 1536 * 2);
  const int RS = ffn ? 3584 : 3072, LSTR = ffn ? 128 : 1024;
  const unsigned lb1 = ldsbase + lw * 1024, lb2 = ldsbase + lw * 2048, lb4 = ldsbase + lw * 4096;
  unsigned sx = ti * 131072 + lw * 32768, sw = 16 * lw * RS + (ffn ? (cg * 448 + (cg & 1) * 64) : cg * 128), ss = 16 * lw * 3584 + cg * 448 + ((cg & 1) ? 0 : 384);
  unsigned vx[2], vw[2], vs;
  v2u ep;
  auto issue_x = [&](int s) {
    OPAQUE_S(sx);
#pragma unroll
    for (int i = 0; i < 4; i++) dma(lb4, slot_base(s % 3) + i * 1024, vx[i & 1], rx, sx + i * 8192 + s * 128);
  };
  auto issue_w = [&](int s) {
    const int sl = slot_base(s % 3);
    OPAQUE_S(ss); OPAQUE_S(sw);
    const unsigned so = sw + s * 64 * RS;
#pragma unroll
    for (int l = 0; l < 3; l++) { dma(lb2, sl + OFF_WM + l * 8192, vw[0], rw, so + l * LSTR); dma(lb2, sl + OFF_WM + l * 8192 + 1024, vw[1], rw, so + l * LSTR); }
    if (ffn) dma(lb1, sl + OFF_WS, vs, rw, ss + s * 64 * 3584);
  };
  if (wid >= 4) {
    for (int p = 0; p < 2; p++) {
      vx[p] = (lane >> 3) * 1024 + (((lane & 7) ^ (4 * p + (lane >> 4))) * 16);
      vw[p] = (8 * p + (lane >> 3)) * RS + (((lane & 7) ^ ((2 * ((lane >> 4) & 1) + p) << 1)) * 16);
    }
    vs = (lane >> 2) * 3584 + (((lane & 3) ^ ((lane >> 5) << 1)) * 16);
    issue_x(0); issue_w(0);
    if (!ffn && lw < 3) dma(lb1 + (lw ? WPHI_OFF - 1024 : GATE_OFF), 0, lane * 16, RSRC(lw ? wphi : gw, lw ? 32 * 256 * 2 : 1024), lw ? cg * 2048 + (lw - 1) * 1024 : 0);
    issue_x(1);
    ep = epoch_load(ws);
  }
  SCHED0();

  const int r4 = JMIX ? 2 * (r >> 4) + (r & 1) : r & 3, r8 = JMIX ? (r & 15) >> 1 : r >> 2;
  const int t64 = xcd * 4 + r4, jc = r8 ^ JSWAP;
  const int rotb = (7 * r4 + ((7 * r8) >> 1)) % 28;
  const int rotc = (2 * r4 + r8) & 6;
  const rsrc_t rws = RSRC(ws, WS_END);
  u64 tag;
  f32x16 acc[2][2];
  const int hh = lane >> 5;
  const int awB0 = (8 * hh + ((lane & 15) >> 2)) * 128 + ((((lane >> 4) & 1) ^ (2 * ((lane >> 3) & 1) + hh)) * 32) + (lane & 3) * 8;
  const int awB1 = awB0 ^ 64;
  const int zb1 = (1 + 3 * lw) * BLK, zb2 = (13 + 5 * lw) * BLK, zb3 = (33 + 6 * lw) * BLK, zb4 = (57 + 4 * lw) * BLK;
  auto zbase = [&](int j) { return j < 3 ? zb1 : (j < 8 ? zb2 : (j < 14 ? zb3 : zb4)); };
  auto zoffs = [&](int j) { return (j < 3 ? j : (j < 8 ? j - 3 : (j < 14 ? j - 8 : j - 14))) * BLK; };
  auto pre_piece = [&](int n) { return n < 2 ? zb1 + 2 * BLK + n * 1024 : (n < 12 ? zb2 + (n - 2) * 1024 : zb3 + (n - 12) * 1024); };
  const unsigned hflag_off = WS_HFLAG + (t64 * 4 + lw) * 128 + (lane & 3) * 8;
  const unsigned hbase = WS_H + t64 * 2 * HST + 28 * lw * 1024;
  auto ktf = [&](int j) { int t = rotb; OPAQUE_S(t); const int k = j + t; return k >= 28 ? k - 28 : k; };
  const unsigned pl = ldsbase + PF_OFF;
  auto att67 = [&](int zb) {
    const unsigned vb0 = (lane >> 3) * 1024 + (((lane & 7) ^ (((lane >> 4) & 1) << 2)) * 16), vb1 = (vb0 + 8192) ^ 32;
#pragma unroll
    for (int e = 0; e < 2; e++) {
      const unsigned so = 8 * lw * 16384 + jc * 128 + ((6 + e + rotc) & 7) * 16384;
      dma(ldsbase + zb, e * BLK, vb0, RWO, so); dma(ldsbase + zb, e * BLK + 1024, vb1, RWO, so);
    }
  };

  if (wid >= 4) {
    const rsrc_t rw2 = RW2, rwo = RWO;
    unsigned vb[2];
    for (int e = 0; e < 2; e++) vb[e] = (8 * e + (lane >> 3)) * 1024 + (((lane & 7) ^ ((2 * ((lane >> 4) & 1) + e) << 1)) * 16);
    const unsigned s2 = 28 * lw * 16384 + jc * 128, so8 = 8 * lw * 16384 + jc * 128;
    auto wblk_own = [&](int j) {
      const unsigned so = s2 + ktf(j) * 16384;
      dma(ldsbase + zbase(j), zoffs(j), vb[0], rw2, so); dma(ldsbase + zbase(j), zoffs(j) + 1024, vb[1], rw2, so);
    };
    auto wblk_c = [&](int j) {
      if (j < 12) { const unsigned so = s2 + ktf(16 + j) * 16384; dma(ldsbase + zbase(j), zoffs(j), vb[0], rw2, so); dma(ldsbase + zbase(j), zoffs(j) + 1024, vb[1], rw2, so); }
      else { const unsigned so = so8 + ((j - 12 + rotc) & 7) * 16384; dma(ldsbase + zbase(j), zoffs(j), vb[0], rwo, so); dma(ldsbase + zbase(j), zoffs(j) + 1024, vb[1], rwo, so); }
    };
    const rsrc_t rta = RSRC(lw == 0 ? x : (lw == 1 ? w1 : (lw == 2 ? wqkv : w2)), lw == 0 ? 2048 * 1024 : (lw == 2 ? 512 * 3072 : 512 * 3584));
    const rsrc_t rtb = RSRC(lw == 0 ? x : (lw == 1 ? w1 : (lw == 2 ? wqkv : wo)), lw == 0 ? 2048 * 1024 : (lw == 2 ? 512 * 3072 : (lw == 1 ? 512 * 3584 : 512 * 1024)));
    const int trow = 192 + r * 2 + ((lane >> 3) & 1);
    const unsigned tst = lw == 0 ? 128 : (lw == 1 ? 64 * 3584 : (lw == 2 ? 64 * 3072 : 14 * 128));
    const unsigned tv0 = lw == 0 ? (lane < 8 ? ((xcd & 1) * 1024 + (xcd >> 1) * 256 + r * 8 + lane) * 1024 + 512 : OOB)
                       : lw == 1 ? ((lane < 16 && (lane & 7) < 7) ? trow * 3584 + (xcd >> 1) * 896 + (lane & 7) * 128 : OOB)
                       : lw == 2 ? ((lane < 16 && (lane & 7) < 6) ? trow * 3072 + ((lane & 7) >> 1) * 1024 + (xcd >> 1) * 256 + (lane & 1) * 128 : OOB)
                                 : (lane < 14 ? (b * 56 + 14 + lane) * 128 : OOB);
    const unsigned tv3 = lw == 3 ? (lane < 16 ? (b * 16 + lane) * 128 : OOB) : tv0 + 3 * tst, tv4 = (lw == 0 || lw == 3) ? OOB : tv0 + 4 * tst;
    auto paceL = [&](int k) { if (k < 3) touch(pl, tv0 + k * tst, rta); else touch(pl, k == 3 ? tv3 : tv4, rtb); };
    WAITV(5);
    BAR();

#pragma unroll
    for (int k = 0; k < 6; k++) {
      issue_w(k + 1); issue_x(k + 2);
      if (k < 5) { paceL(k); WAITV(5); } else WAITV(4);
      if (k == 0) { asm volatile("" : "+v"(ep)); if (lw == 0) *(volatile LAS v2u*)(L + TAG_OFF) = ep; }
      BAR();
    }
    issue_w(7);
#pragma unroll
    for (int e = 0; e < 4; e++) wblk_own(e);
    WAITV(8);
    BAR();
    tag = epoch_tag(L);
    auto tail = [&](auto ns_) {
    constexpr int NS = decltype(ns_)::value, W0 = NS > 16 ? LWQ : LW0;
    s16x8 wr[16][2];
    auto wread = [&](int j) { wr[j][0] = trfrag(L + zbase(j) + awB0, zoffs(j), zoffs(j) + 512); wr[j][1] = trfrag(L + zbase(j) + awB1, zoffs(j), zoffs(j) + 512); };
#pragma unroll
    for (int e = 4; e < 14; e++) wblk_own(e);
    WAITV(12);
#pragma unroll
    for (int e = 0; e < 8; e++) wread(e);
    WAITL(); SCHED0();
#pragma unroll
    for (int j = 0; j < 4; j++) wblk_c(j);
    sy_wait4_s(L, SY_DONE);

    wblk_own(14); wblk_own(15);
#pragma unroll
    for (int j = 4; j < 8; j++) wblk_c(j);
    WAITV(20);

#pragma unroll
    for (int e = 8; e < 14; e++) wread(e);
    WAITL(); SCHED0();
    wblk_c(8); wblk_c(9);
    WAITV(12);

    wread(14); wread(15);
    SCHED0();
    wblk_c(10); wblk_c(11);
    if (NS > 16) { wblk_c(12); wblk_c(13); }
    WAITL(); SCHED0();
    if (NS > 16) {
      wblk_c(14); wblk_c(15);
      sy_wait_s(L, SY_XR + (lw ^ 1), 1);
      wblk_c(16); wblk_c(17);
    }
    {
      u64 v = __hip_atomic_load((const GAS u64*)(ws + hflag_off), RLX_AGENT);
      WAITV(1);
      if (NS == 16) sy_set(L, SY_WA + lw, 1);
      asm volatile("s_waitcnt vmcnt(0)" : "+v"(v) :: "memory");
      if (NS > 16) sy_set(L, SY_WB + lw, 1);
      if (!__all(v == tag)) poll_flags((const GAS u64*)(ws + hflag_off), tag);
      __builtin_amdgcn_fence(__ATOMIC_ACQUIRE, "wavefront");
      asm volatile("" ::: "memory");
    }
    acc[0][0] = zero16(); acc[0][1] = zero16(); acc[1][0] = zero16(); acc[1][1] = zero16();
    {
      v4u av[NS][2];
      auto ldA = [&](int e) { const unsigned so = hbase + ktf(e) * 1024; av[e][0] = ld_sc1(rws, lane * 16, so); av[e][1] = ld_sc1(rws, lane * 16, so + HST); };
#pragma unroll
      for (int e = 0; e < W0; e++) ldA(e);
      SCHED0();
#pragma unroll
      for (int j = 0; j < NS; j++) {
        s16x8 w0, w1f;
        if (j < 16) { w0 = wr[j < 16 ? j : 0][0]; w1f = wr[j < 16 ? j : 0][1]; }
        else { w0 = trfrag(L + zbase(j - 16) + awB0, zoffs(j - 16), zoffs(j - 16) + 512); w1f = trfrag(L + zbase(j - 16) + awB1, zoffs(j - 16), zoffs(j - 16) + 512); }
        acc[0][0] = mfma32(w0, av[j][0], acc[0][0]);
        acc[1][0] = mfma32(w1f, av[j][0], acc[1][0]);
        acc[0][1] = mfma32(w0, av[j][1], acc[0][1]);
        acc[1][1] = mfma32(w1f, av[j][1], acc[1][1]);
        constexpr auto lim = [](int j) { return NS > 16 ? W0 + (GROW * j) / 2 : W0 + 2 * j; };
        if (lim(j) < NS) {
          SCHED0();
#pragma unroll
          for (int n = lim(j); n < lim(j + 1); n++)
            if (n < NS) ldA(n);
          SCHED0();
        }
        if (NS == 16 && j >= ADEF && j < ADEF + 3) { SCHED0(); wblk_c(12 + 2 * (j - ADEF)); wblk_c(13 + 2 * (j - ADEF)); SCHED0(); }
        if (NS == 16 && j == 13) { SCHED0(); WAITV(0); sy_set(L, SY_WB + lw, 1); SCHED0(); }
      }
    }
    };
    if (ffn) tail(std::integral_constant<int, 16>{});
    else tail(std::integral_constant<int, 28>{});
    if (b == 0 && lw == 3) {
      const GAS char* hf = (const GAS char*)ws + WS_HFLAG + ((lane >> 3) * 8 + ((lane & 7) >> 1)) * 128 + (lane & 1) * 16;
      const GAS char* kf = (const GAS char*)ws + WS_KFLAG + (lane >> 4) * 512 + (lane & 15) * 8;
#pragma unroll 1
      for (unsigned spins = 0; spins < SPIN_CAP; ++spins) {
        const u64 v0 = __hip_atomic_load((const GAS u64*)hf, RLX_AGENT), v1 = __hip_atomic_load((const GAS u64*)(hf + 8192), RLX_AGENT);
        const u64 v2 = __hip_atomic_load((const GAS u64*)kf, RLX_AGENT), v3 = __hip_atomic_load((const GAS u64*)(kf + 2048), RLX_AGENT);
        if (__all(v0 == tag && v1 == tag && v2 == tag && v3 == tag)) break;
        __builtin_amdgcn_s_sleep(1);
      }
      if (lane == 0) __hip_atomic_store((GAS u64*)(ws + WS_EPOCH), tag, RLX_AGENT);
    }
    if (ffn) {
      sy_wait_s(L, SY_CF + lw, 1);
#pragma unroll
      for (int n = 0; n < 16; n++) {
        const f32x4 v = *(const LAS f32x4*)(L + lane * 16 + pre_piece(n));
        f32x16& t = acc[n >> 3][(n >> 2) & 1];
#pragma unroll
        for (int i = 0; i < 4; i++) t[4 * (n & 3) + i] += v[i];
      }
      WAITL(); SCHED0();
    }
  } else {
    const int m = lw & 1, nh = lw >> 1, q = lane >> 4;
    if (lw == 0) *(LAS unsigned*)(L + SYNC + lane * 4) = 0;
    { const int row = (1 + (lane >> 4)) * 64 + r * 2 + ((lane >> 3) & 1);
      if (lw == 0) touch(pl, lane < 8 ? ((xcd & 1) * 1024 + (xcd >> 1) * 256 + r * 8 + lane) * 1024 + 3 * 128 : OOB, RX);
      else if (lw == 1) touch(pl, (lane < 32 && (lane & 7) < 7) ? row * 3584 + (xcd >> 1) * 896 + (lane & 7) * 128 : OOB, RW1);
      else if (lw == 2) touch(pl, (lane < 32 && (lane & 7) < 6) ? row * 3072 + ((lane & 7) >> 1) * 1024 + (xcd >> 1) * 256 + (lane & 1) * 128 : OOB, RWQ);
      else touch(pl, lane < 14 ? (b * 56 + lane) * 128 : OOB, RW2); }
    GAS u64* own_flag;
    v4u av[12][2];
    auto load_h = [&](int j0, int j1) {
#pragma unroll
      for (int j = j0; j < j1; j++) { const unsigned so = hbase + ktf(16 + j) * 1024; av[j][0] = ld_sc1(rws, lane * 16, so); av[j][1] = ld_sc1(rws, lane * 16, so + HST); }
    };
    s16x8 wa[8][2];
    auto rd67 = [&](int zb) {
#pragma unroll
      for (int j = 6; j < 8; j++) { wa[j][0] = trfrag(L + zb + awB0, (j - 6) * BLK, (j - 6) * BLK + 512); wa[j][1] = trfrag(L + zb + awB1, (j - 6) * BLK, (j - 6) * BLK + 512); }
    };
    auto rd05 = [&]() {
      sy_wait_s(L, SY_WB + lw, 1);
#pragma unroll
      for (int j = 0; j < 6; j++) { wa[j][0] = trfrag(L + zbase(12 + j) + awB0, zoffs(12 + j), zoffs(12 + j) + 512); wa[j][1] = trfrag(L + zbase(12 + j) + awB1, zoffs(12 + j), zoffs(12 + j) + 512); }
    };
    const int fA = 2 * ((lane >> 3) & 1) + (q & 1), rbA = 8 * q + ((lane & 15) >> 2);
    const int ax0 = slot_base(0) + m * 8192 + (lane & 15) * 128 + ((q ^ ((lane & 15) >> 1)) * 16);
    int d0 = slot_base(1) - slot_base(0), d1 = slot_base(2) - slot_base(1), d2 = slot_base(0) - slot_base(2);
    if (ffn) {
      LAS char *px0 = L + ax0, *px1 = L + (ax0 ^ 64), *pw[4], *ps;
      for (int i = 0; i < 4; i++) { const int T = 6 * nh + i; pw[i] = L + slot_base(0) + OFF_WM + rbA * 128 + (lane & 3) * 8 + (((T & 3) ^ fA) * 32) + (T >> 2) * 8192; }
      ps = L + slot_base(0) + OFF_WS + rbA * 64 + ((nh ^ (q & 1)) * 32) + (lane & 3) * 8;
      f32x4 ca[7][4];
      for (int i = 0; i < 7; i++) for (int j = 0; j < 4; j++) ca[i][j] = f32x4{0, 0, 0, 0};
      s16x8 xf[2][4], wf[2][7];
      auto rd = [&](int u, int bi) {
#pragma unroll
        for (int t = 0; t < 4; t++) xf[bi][t] = *(LAS s16x8*)((u ? px1 : px0) + t * 2048);
#pragma unroll
        for (int i = 0; i < 6; i++) wf[bi][i] = trfrag(pw[i & 3], u * 4096 + (i >> 2) * 8192, u * 4096 + 512 + (i >> 2) * 8192);
        wf[bi][6] = trfrag(ps, u * 2048, u * 2048 + 256);
      };
      auto mm = [&](int bi) {
#pragma unroll
        for (int i = 0; i < 7; i++)
#pragma unroll
          for (int t = 0; t < 4; t++) ca[i][t] = mfma16(wf[bi][i], xf[bi][t], ca[i][t]);
      };
      BAR();
      rd(0, 0);
#pragma unroll 1
      for (int s = 0; s < 7; s++) {
        rd(1, 1);
        mm(0);
        px0 += d0; px1 += d0; ps += d0;
#pragma unroll
        for (int i = 0; i < 4; i++) pw[i] += d0;
#pragma unroll
        for (int i = 0; i < 18; i++) { SGB(0x8, 1); SGB(0x100, 1); }
#pragma unroll
        for (int i = 0; i < 7; i++) { SGB(0x8, 1); SGB(0x2, 1); }
        SGB(0x8, 3);
        { const int t = d0; d0 = d1; d1 = d2; d2 = t; }
        asm volatile("" : "+v"(px0), "+v"(px1), "+v"(ps), "+v"(pw[0]), "+v"(pw[1]), "+v"(pw[2]), "+v"(pw[3]));
        SCHED0();
        BAR();
        rd(0, 0); mm(1);
#pragma unroll
        for (int i = 0; i < 18; i++) { SGB(0x8, 1); SGB(0x100, 1); }
        SGB(0x8, 10);
        SCHED0();
      }
      rd(1, 1); mm(0);
#pragma unroll
      for (int i = 0; i < 18; i++) { SGB(0x8, 1); SGB(0x100, 1); }
      SGB(0x8, 10);
      SCHED0();
      sy_set(L, SY_DONE + lw, 1);
      tag = epoch_tag(L);
      SCHED0();
      const int kt_main = 14 * cg + ((cg & 1) ? 2 : 0) + 6 * nh, kt_side = 14 * cg + ((cg & 1) ? 0 : 12) + nh;
      const unsigned hrow = WS_H + (ti * 4 + m * 2) * HST;
#pragma unroll
      for (int i = 0; i < 8; i++) {
        if (i < 7) {
#pragma unroll
          for (int t = 0; t < 4; t++) ca[i][t] = mfma16(wf[1][i], xf[1][t], ca[i][t]);
        }
        if (i > 0) {
          const int kt = (i - 1 < 6) ? kt_main + i - 1 : kt_side;
#pragma unroll
          for (int a = 0; a < 2; a++) {
            const v4u f = to_frag32(ca[i - 1][2 * a], ca[i - 1][2 * a + 1]);
            st_sc1(v4u{relu2(f[0]), relu2(f[1]), relu2(f[2]), relu2(f[3])}, rws, lane * 16, hrow + a * HST + kt * 1024);
          }
        }
        if (i > 0 && i < 7) {
#pragma unroll
          for (int t = 0; t < 4; t++) { SGB(0x8, 1); SGB(0x2, 5); }
        }
        SCHED0();
      }
      own_flag = (GAS u64*)(ws + WS_HFLAG + ((ti * 2 + m) * 4 + (cg >> 1)) * 128 + ((cg & 1) * 2 + nh) * 8);
    } else {
      const int p = nh;
      LAS char *pxp0 = L + ax0 + p * 4096, *pxp1 = L + (ax0 ^ 64) + p * 4096, *pxn0 = L + ax0 + (1 - p) * 4096, *pxn1 = L + (ax0 ^ 64) + (1 - p) * 4096, *pw[2];
      for (int i = 0; i < 2; i++) pw[i] = L + slot_base(0) + OFF_WM + rbA * 128 + (lane & 3) * 8 + (((2 * p + (i ^ m)) ^ fA) * 32);
      LAS char* pg = L + GATE_OFF + q * 16;
      v4u idn[2], sel[2];
      make_idn(idn, lane);
      f32x4 cq[2][4], ck[2][4], cv[2][4], cgate[2];
      for (int i = 0; i < 2; i++) { cgate[i] = f32x4{0, 0, 0, 0}; for (int t = 0; t < 4; t++) { cq[i][t] = cgate[i]; ck[i][t] = cgate[i]; cv[i][t] = cgate[i]; } }
      s16x8 xf[2][4], wf[2][6], gf[2];
      auto rd = [&](int u, int bi) {
#pragma unroll
        for (int t = 0; t < 4; t++) xf[bi][t] = *(LAS s16x8*)((t < 2 ? (u ? pxp1 : pxp0) : (u ? pxn1 : pxn0)) + (t & 1) * 2048);
#pragma unroll
        for (int i = 0; i < 6; i++) { const int l = (i >> 1) == 2 ? 0 : (i >> 1) + 1; wf[bi][i] = trfrag(pw[i & 1], u * 4096 + l * 8192, u * 4096 + 512 + l * 8192); }
        gf[bi] = *(LAS s16x8*)(pg + u * 64);
      };
      auto mm = [&](int bi) {
#pragma unroll
        for (int i = 0; i < 2; i++)
#pragma unroll
          for (int t = 0; t < 4; t++) ck[i][t] = mfma16(wf[bi][i], xf[bi][t], ck[i][t]);
#pragma unroll
        for (int i = 0; i < 2; i++)
#pragma unroll
          for (int t = 0; t < 4; t++) cv[i][t] = mfma16(xf[bi][t], wf[bi][2 + i], cv[i][t]);
#pragma unroll
        for (int i = 0; i < 2; i++)
#pragma unroll
          for (int t = 0; t < 4; t++) cq[i][t] = mfma16(wf[bi][4 + i], xf[bi][t], cq[i][t]);
        cgate[0] = mfma16(gf[bi], xf[bi][0], cgate[0]);
        cgate[1] = mfma16(gf[bi], xf[bi][1], cgate[1]);
      };
      BAR();
      rd(0, 0);
      const v4u wp[2] = {make_wp(L + WPHI_OFF + lw * 512, lane), make_wp(L + WPHI_OFF + (lw ^ 1) * 512, lane)};
      SCHED0();
#pragma unroll 1
      for (int s = 0; s < 7; s++) {
        rd(1, 1);
        mm(0);
        pxp0 += d0; pxp1 += d0; pxn0 += d0; pxn1 += d0; pw[0] += d0; pw[1] += d0; pg += 128;
#pragma unroll
        for (int i = 0; i < 17; i++) { SGB(0x8, 1); SGB(0x100, 1); }
#pragma unroll
        for (int i = 0; i < 7; i++) { SGB(0x8, 1); SGB(0x2, 1); }
        SGB(0x8, 2);
        { const int t = d0; d0 = d1; d1 = d2; d2 = t; }
        asm volatile("" : "+v"(pxp0), "+v"(pxp1), "+v"(pxn0), "+v"(pxn1), "+v"(pw[0]), "+v"(pw[1]), "+v"(pg));
        SCHED0();
        BAR();
        rd(0, 0); mm(1);
#pragma unroll
        for (int i = 0; i < 17; i++) { SGB(0x8, 1); SGB(0x100, 1); }
        SGB(0x8, 9);
        SCHED0();
      }
      rd(1, 1); mm(0);
#pragma unroll
      for (int i = 0; i < 17; i++) { SGB(0x8, 1); SGB(0x100, 1); }
      SGB(0x8, 9);
      SCHED0();
      sy_set(L, SY_DONE + lw, 1);
      tag = epoch_tag(L);
      SCHED0();
      const int hd = 4 * cg + 2 * p + m;
      {
        f32x16 P[4]; v4u ph[4][2], vb[4], xo[4];
        mm(1);
#pragma unroll
        for (int u = 0; u < 2; u++) { xo[u] = to_frag32(ck[1][2 * u], ck[1][2 * u + 1]); xo[2 + u] = pack44(cv[1][2 * u], cv[1][2 * u + 1]); }
#pragma unroll
        for (int u = 0; u < 2; u++) { P[u] = mfma32(wp[0], to_frag32(ck[0][2 * u], ck[0][2 * u + 1]), zero16()); vb[u] = pack44(cv[0][2 * u], cv[0][2 * u + 1]); }
        SGB(0x8, 10);
#pragma unroll
        for (int i = 0; i < 8; i++) { SGB(0x8, 1); SGB(0x2, 3); }
#pragma unroll
        for (int i = 0; i < 8; i++) { SGB(0x8, 1); SGB(0x2, 2); }
        SGB(0x8, 2);
        SCHED0();
        sy_wait4_s(L, SY_DONE);
#pragma unroll
        for (int u = 0; u < 4; u++) *(LAS v4u*)(L + zb4 + 2 * BLK + u * 1024 + lane * 16) = xo[u];
        *(LAS float*)(L + SCR_GX + lw * 128 + (lane & 31) * 4) = (lane & 16) ? cgate[1][0] : cgate[0][0];
        SCHED0();
        const v4u ones = {0x3f803f80u, 0x3f803f80u, 0x3f803f80u, 0x3f803f80u};
        const f32x4 z4 = {0, 0, 0, 0};
        f32x4 Z[4][2][2], kv[2], zz[2];
        auto transp = [&](int u) {
          v4u a0, a1;
#pragma unroll
          for (int i = 0; i < 4; i++) { const auto sw_ = __builtin_amdgcn_permlane16_swap(ph[u][0][i], ph[u][1][i], false, false); a0[i] = sw_[0]; a1[i] = sw_[1]; }
#pragma unroll
          for (int v = 0; v < 2; v++) { Z[u][0][v] = mfma16(a0, idn[v], z4); Z[u][1][v] = mfma16(a1, idn[v], z4); }
        };
        auto accum = [&](int u) {
#pragma unroll
          for (int v = 0; v < 2; v++) {
            const v4u a = pack44(Z[u][0][v], Z[u][1][v]);
            kv[v] = mfma16(a, vb[u], u ? kv[v] : z4);
            zz[v] = mfma16(a, ones, u ? zz[v] : z4);
          }
        };
        softmax16<2>(P, ph, [&]() { sy_set(L, SY_XCH + lw, 1); });
        SCHED0();
        sy_wait(L, SY_XCH + (lw ^ 1), 1);
        const LAS char* xr = L + (57 + 4 * (lw ^ 1) + 2) * BLK + lane * 16;
#pragma unroll
        for (int u = 0; u < 4; u++) xo[u] = *(const LAS v4u*)(xr + u * 1024);
        transp(0); transp(1);
        SCHED0();
        P[2] = mfma32(wp[0], xo[0], zero16()); P[3] = mfma32(wp[0], xo[1], zero16());
        vb[2] = xo[2]; vb[3] = xo[3];
        accum(0); accum(1);
        SCHED0();
        sy_set(L, SY_XR + lw, 1);
        softmax16<2>(P + 2, ph + 2);
        transp(2); transp(3);
        SCHED0();
        accum(2); accum(3);
        st_sc1(pack44(kv[0], kv[1]), rws, lane * 16, WS_KVP + (hd * 16 + ti) * 1024);
        st_sc1(pack44(zz[0], zz[1]), rws, (lane & 15) == 0 ? q * 16 : OOB, WS_ZP + (hd * 16 + ti) * 128);
      }
      SCHED0();
      v4u pq[4][2];
      {
        f32x16 P[4];
#pragma unroll
        for (int u = 0; u < 4; u++) P[u] = mfma32(wp[u & 1], to_frag32(cq[u & 1][u & 2], cq[u & 1][(u & 2) + 1]), zero16());
#pragma unroll
        for (int u = 0; u < 2; u++)
#pragma unroll
          for (int jj = 0; jj < 4; jj++) {
            const int f0 = 16 * (jj >> 1) + 4 * q + 2 * (jj & 1), rho = lane & 15;
            sel[u][jj] = ((f0 == 16 * u + rho) ? 0x3f80u : 0u) | ((f0 + 1 == 16 * u + rho) ? 0x3f800000u : 0u);
          }
        asm volatile("" : "+v"(sel[0]), "+v"(sel[1]));
        softmax16<1>(P, pq);
        asm volatile("" : "+v"(pq[0][0]), "+v"(pq[0][1]), "+v"(P[1]), "+v"(P[2]), "+v"(P[3]));
        WAITV(0);
        if (lane == 0) __hip_atomic_store((GAS u64*)(ws + WS_KFLAG + hd * 128 + ti * 8), tag, RLX_AGENT);
        asm volatile("" : "+v"(P[1]), "+v"(P[2]), "+v"(P[3]));
        softmax16<3>(P + 1, pq + 1);
#pragma unroll
        for (int u = 1; u < 4; u++) asm volatile("" : "+v"(pq[u][0]), "+v"(pq[u][1]));
      }
      sy_wait(L, SY_XCH + (lw ^ 2), 1);
      float gt[2];
      { const float l0 = *(const LAS float*)(L + SCR_GX + lw * 128 + (lane & 31) * 4), l1 = *(const LAS float*)(L + SCR_GX + (lw ^ 2) * 128 + (lane & 31) * 4);
        gt[0] = __builtin_amdgcn_rcpf(1.f + __builtin_amdgcn_exp2f(-1.4426950408889634f * l0));
        gt[1] = __builtin_amdgcn_rcpf(1.f + __builtin_amdgcn_exp2f(-1.4426950408889634f * l1));
        asm volatile("" : "+v"(gt[0]), "+v"(gt[1])); }
      {
        const unsigned go = ((cg & 1) == 0 && p == 0) ? (WS_G + ((ti * 2 + m) * 4 + (cg >> 1)) * 256 + lane * 4) : OOB;
        __builtin_amdgcn_raw_buffer_store_b32(__float_as_uint(1.f - (hh ? gt[1] : gt[0])), rws, go, 0, 16);
      }
      rd05(); WAITL(); SCHED0();
      att67(zb4);
      {
        const int rho = lane & 15;
        poll_flags((const GAS u64*)(ws + WS_KFLAG + hd * 128 + (lane & 15) * 8), tag);
        v4u kp[16];
        constexpr int GW = GWIN;
        unsigned ko = WS_KVP + hd * 16384, kt = ti;
        auto ldk = [&](int i) { OPAQUE_S(kt); kp[i] = ld_sc1(rws, lane * 16, ko + ((i + kt) & 15) * 1024); SCHED0(); };
#pragma unroll
        for (int i = 0; i < GW; i++) ldk(i);
        const v4u zp = ld_sc1(rws, (lane & 15) * 128 + q * 16, WS_ZP + hd * 2048);
        SCHED0();
        LAS char* ks = L + SCR_KS + lw * 1088;
        f32x4 t0 = {0, 0, 0, 0}, t1 = {0, 0, 0, 0};
        float z[8];
#pragma unroll
        for (int i = 0; i < 16; i++) {
          if (GW + i < 16) ldk(GW + i);
          t0 = mfma16(sel[0], kp[i], t0); t1 = mfma16(sel[1], kp[i], t1);
          if (i >= 8) z[i - 8] = row_sum16(__uint_as_float((i & 1) ? zp[(i - 8) >> 1] & 0xffff0000u : zp[(i - 8) >> 1] << 16));
          SCHED0();
        }
        if (rho == 0) {
          *(LAS v2u*)(ks + 1024 + q * 8) = v2u{pk(z[0], z[1]), pk(z[2], z[3])};
          *(LAS v2u*)(ks + 1024 + q * 8 + 32) = v2u{pk(z[4], z[5]), pk(z[6], z[7])};
        }
        *(LAS v2u*)(ks + rho * 64 + q * 8) = v2u{pk(t0[0], t0[1]), pk(t0[2], t0[3])};
        *(LAS v2u*)(ks + rho * 64 + 32 + q * 8) = v2u{pk(t1[0], t1[1]), pk(t1[2], t1[3])};
        sy_set(L, SY_KS + lw, 1);
      }
      { const int rho = lane & 31, krow = rho < 16 ? (8 * ((rho >> 2) & 1) + (rho & 3) + 4 * (rho >> 3)) : 16;
        const unsigned orow = WS_O + (ti * 4 + m * 2) * OST + (4 * cg + 2 * p) * 1024;
        sy_wait(L, SY_KS + (lw ^ 1), 1);
        f32x16 o[4];
#pragma unroll
        for (int s = 0; s < 2; s++) {
          const LAS char* ks = L + SCR_KS + (lw ^ s) * 1088 + krow * 64 + hh * 32;
          const v4u k0 = *(const LAS v4u*)ks, k1 = *(const LAS v4u*)(ks + 16);
#pragma unroll
          for (int t = 0; t < 2; t++) { o[2 * t + s] = mfma32(k0, pq[2 * t + s][0], zero16()); o[2 * t + s] = mfma32(k1, pq[2 * t + s][1], o[2 * t + s]); }
        }
#pragma unroll
        for (int s = 0; s < 2; s++)
#pragma unroll
          for (int t = 0; t < 2; t++) {
            f32x16& oo = o[2 * t + s];
            const float sc = gt[t] * __builtin_amdgcn_rcpf(oo[8] + 1e-6f);
#pragma unroll
            for (int i = 0; i < 8; i++) oo[i] *= sc;
            st_sc1(pack8(oo, 0), rws, lane * 16, orow + (t ^ p) * OST + (s ^ m) * 1024);
          }
        own_flag = (GAS u64*)(ws + WS_OFLAG + ((ti * 2 + m) * 4 + (cg >> 1)) * 128 + ((cg & 1) * 2 + p) * 8);
      }
    }
    const GAS u64* ofp = (const GAS u64*)(ws + WS_OFLAG + (t64 * 4 + lw) * 128 + (((lane & 3) + (rotc >> 1)) & 3) * 8);
    u64 ofl = 0;
    if (ffn) {
      publish_then_poll(own_flag, (const GAS u64*)(ws + hflag_off), tag, lane);
      load_h(0, 12);
      SCHED0();
      sy_wait(L, SY_WA + lw, 1);
      acc[0][0] = zero16(); acc[0][1] = zero16(); acc[1][0] = zero16(); acc[1][1] = zero16();
#pragma unroll
      for (int j = 0; j < 12; j++) {
        const s16x8 w0 = trfrag(L + zbase(j) + awB0, zoffs(j), zoffs(j) + 512), w1f = trfrag(L + zbase(j) + awB1, zoffs(j), zoffs(j) + 512);
        acc[0][0] = mfma32(w0, av[j][0], acc[0][0]);
        acc[1][0] = mfma32(w1f, av[j][0], acc[1][0]);
        acc[0][1] = mfma32(w0, av[j][1], acc[0][1]);
        acc[1][1] = mfma32(w1f, av[j][1], acc[1][1]);
        if (j == 3) { SCHED0(); WAITL(); att67(zb1); SCHED0(); }
      }
      SCHED0();
      WAITV(0);
      ofl = __hip_atomic_load(ofp, RLX_AGENT);
      WAITL(); SCHED0();
#pragma unroll
      for (int n = 0; n < 16; n++) {
        const f32x16& t = acc[n >> 3][(n >> 2) & 1];
        *(LAS f32x4*)(L + lane * 16 + pre_piece(n)) = f32x4{t[4 * (n & 3)], t[4 * (n & 3) + 1], t[4 * (n & 3) + 2], t[4 * (n & 3) + 3]};
      }
      sy_set(L, SY_CF + lw, 1);
      rd67(zb1); rd05();
    } else {
      WAITV(0);
      if (lane == 0) __hip_atomic_store(own_flag, tag, RLX_AGENT);
      rd67(zb4);
    }
    v4u ov[8][2];
    float gv = 0.f;
    const unsigned obase = WS_O + t64 * 2 * OST + 8 * lw * 1024;
    unsigned ready = (unsigned)__ballot(ofl == tag) & 15u;
    acc[0][0] = zero16(); acc[0][1] = zero16(); acc[1][0] = zero16(); acc[1][1] = zero16();
#pragma unroll
    for (int f = 0; f < 5; f++) {
      if (f < 4) {
        if (!(ready & (1u << f))) {
#pragma unroll 1
          for (unsigned spins = 0; spins < SPIN_CAP; ++spins) {
            ready = (unsigned)__ballot(__hip_atomic_load(ofp, RLX_AGENT) == tag) & 15u;
            if (ready & (1u << f)) break;
            if (POLLS) __builtin_amdgcn_s_sleep(POLLS);
          }
        }
        __builtin_amdgcn_fence(__ATOMIC_ACQUIRE, "wavefront");
        asm volatile("" ::: "memory");
        if (f == 3) { gv = __uint_as_float(__builtin_amdgcn_raw_buffer_load_b32(rws, lane * 4, WS_G + (t64 * 4 + lw) * 256, 16)); }
#pragma unroll
        for (int j = 2 * f; j < 2 * f + 2; j++) { const unsigned so = obase + ((j + rotc) & 7) * 1024; ov[j][0] = ld_sc1(rws, lane * 16, so); ov[j][1] = ld_sc1(rws, lane * 16, so + OST); }
        SCHED0();
      }
      if (f == 4) {
        if (lw == 0) *(LAS float*)(L + SCR_G2 + lane * 4) = gv;
        SCHED0();
      }
      if (f > 0) {
#pragma unroll
        for (int j = 2 * f - 2; j < 2 * f; j++) {
          acc[0][0] = mfma32(wa[j][0], ov[j][0], acc[0][0]);
          acc[1][0] = mfma32(wa[j][1], ov[j][0], acc[1][0]);
          acc[0][1] = mfma32(wa[j][0], ov[j][1], acc[0][1]);
          acc[1][1] = mfma32(wa[j][1], ov[j][1], acc[1][1]);
        }
        SCHED0();
      }
    }
    SCHED0();
  }

  {
    const int half = wid >> 2;
    if (half) {
#pragma unroll
      for (int n = 0; n < 16; n++) {
        const f32x16& t = acc[n >> 3][(n >> 2) & 1];
        *(LAS f32x4*)(L + lane * 16 + pre_piece(n)) = f32x4{t[4 * (n & 3)], t[4 * (n & 3) + 1], t[4 * (n & 3) + 2], t[4 * (n & 3) + 3]};
      }
    } else {
#pragma unroll
      for (int n = 0; n < 8; n++) *(LAS v4u*)(L + zb4 + n * 1024 + lane * 16) = pack8(acc[n >> 2][(n >> 1) & 1], n & 1);
    }
    const LAS char* rc = L + lane * 16 + 57 * BLK + (lw * 2 + half) * 1024;
    const int n0 = lw * 4 + half * 2, sl = n0 < 2 ? 3 * BLK : (n0 < 12 ? 5 * BLK : 6 * BLK);
    const LAS char* rl = L + lane * 16 + (n0 < 2 ? 3 * BLK : (n0 < 12 ? 13 * BLK + (n0 - 2) * 1024 : 33 * BLK + (n0 - 12) * 1024));
    const LAS char *rl1 = rl + sl, *rl2 = rl + 2 * sl, *rl3 = rl + 3 * sl;
    const LAS char* rg = L + SCR_G2 + (lw & 1) * 128 + (lane & 31) * 4;
    asm volatile("" : "+v"(rc), "+v"(rl), "+v"(rl1), "+v"(rl2), "+v"(rl3), "+v"(rg));
    BAR();
    v4u cp[4];
    const f32x4 a0_ = *(const LAS f32x4*)rl, a1_ = *(const LAS f32x4*)rl1, a2_ = *(const LAS f32x4*)rl2, a3_ = *(const LAS f32x4*)rl3;
    const f32x4 b0_ = *(const LAS f32x4*)(rl + 1024), b1_ = *(const LAS f32x4*)(rl1 + 1024), b2_ = *(const LAS f32x4*)(rl2 + 1024), b3_ = *(const LAS f32x4*)(rl3 + 1024);
    const float g = *(const LAS float*)rg;
#pragma unroll
    for (int i = 0; i < 4; i++) cp[i] = *(const LAS v4u*)(rc + i * 4 * BLK);
    SCHED0();
    const f32x4 l0 = (a0_ + a1_) + (a2_ + a3_), l1 = (b0_ + b1_) + (b2_ + b3_);
    f32x4 c0 = l0 * g, c1 = l1 * g;
#pragma unroll
    for (int i = 0; i < 4; i++) {
      c0 += f32x4{__uint_as_float(cp[i][0] << 16), __uint_as_float(cp[i][0] & 0xffff0000u), __uint_as_float(cp[i][1] << 16), __uint_as_float(cp[i][1] & 0xffff0000u)};
      c1 += f32x4{__uint_as_float(cp[i][2] << 16), __uint_as_float(cp[i][2] & 0xffff0000u), __uint_as_float(cp[i][3] << 16), __uint_as_float(cp[i][3] & 0xffff0000u)};
    }
    const unsigned a0 = pk(c0[0], c0[1]), a1 = pk(c0[2], c0[3]), b0 = pk(c1[0], c1[1]), b1 = pk(c1[2], c1[3]);
    const auto r0 = __builtin_amdgcn_permlane32_swap(a0, b0, false, false);
    const auto r1 = __builtin_amdgcn_permlane32_swap(a1, b1, false, false);
    const int tok = t64 * 64 + (lw & 1) * 32 + (lane & 31);
    const int col = jc * 64 + (lw >> 1) * 32 + half * 16 + hh * 8;
    __builtin_amdgcn_raw_buffer_store_b128(v4u{r0[0], r1[0], r0[1], r1[1]}, RSRC(out, 2048 * 512 * 2), (tok * 512 + col) * 2, 0, 0);
  }

}

extern "C" void kernel_launch(void* const* d_in, const int* in_sizes, int n_in, void* d_out, int out_size, void* d_ws, size_t ws_size,
                              hipStream_t stream) {
  (void)hipFuncSetAttribute((const void*)fused_kernel, hipFuncAttributeMaxDynamicSharedMemorySize, LDS_BYTES);
  fused_kernel<<<256, 512, LDS_BYTES, stream>>>((const u16*)d_in[0], (const u16*)d_in[1], (const u16*)d_in[2], (const u16*)d_in[3],
                                                (const u16*)d_in[5], (const u16*)d_in[6], (const u16*)d_in[7], (u16*)d_out, (char*)d_ws);
}
```
